```python
import torch

def _softmax(x, axis=-1):
    return torch.softmax(x, dim=axis)
def _gelu(x):
    return torch.nn.functional.gelu(x, approximate="tanh")
def _silu(x):
    return torch.nn.functional.silu(x)
def _sigmoid(x):
    return torch.sigmoid(x)
def _layernorm(x, axis=-1, eps=1e-5):
    m = x.mean(dim=axis, keepdim=True)
    v = x.var(dim=axis, keepdim=True, unbiased=False)
    return (x - m) / torch.sqrt(v + eps)
def _scatter_add(tgt, idx, src, axis=0):
    return tgt.clone().index_add_(axis, idx, src)
def _segment_sum(src, seg, n):
    out = torch.zeros((n,) + src.shape[1:], dtype=src.dtype)
    return out.index_add_(0, seg, src)
def _pad(x, pw):
    flat = tuple(v for pair in reversed(pw) for v in pair)
    return torch.nn.functional.pad(x, flat)

kernel_name = 'concat_heads__mha_block_cached__gqa_block_cached__decode'

def setup_inputs():

    _w = (lambda a: (lambda t: t.to(torch.bfloat16) if t.is_floating_point() else t)(torch.as_tensor(a)))
    def _A_setup():
        B, Sq, D = 1, 1, 640
        H, Sk, Dh = 32, 8192, 128
        return {
            "x": _w((lambda *s: torch.randn(*s, dtype=torch.bfloat16) * 0.8)(B, Sq, D)),
            "k_cache": _w((lambda *s: torch.randn(*s, dtype=torch.bfloat16) * 1.6)(B, H, Sk, Dh)),
            "v_cache": _w((lambda *s: torch.randn(*s, dtype=torch.bfloat16) * 0.2)(B, H, Sk, Dh)),
            "wq": _w((lambda *s: torch.randn(*s, dtype=torch.bfloat16) * 1.6)(D, H * Dh) * (D ** -0.5)),
            "wo": _w((lambda *s: torch.randn(*s, dtype=torch.bfloat16) * 0.2)(H * Dh, D) * ((H * Dh) ** -0.5)),
        }
    _A_in = _A_setup()

    def _B_setup():
        B, Sq, D = 1, 1, 640
        Hq, Hkv, Sk, Dh = 32, 32, 8192, 128
        assert Hq % Hkv == 0, (Hq, Hkv)
        return {
            "x": _w((lambda *s: torch.randn(*s, dtype=torch.bfloat16) * 0.8)(B, Sq, D)),
            "k_cache": _w((lambda *s: torch.randn(*s, dtype=torch.bfloat16) * 1.6)(B, Hkv, Sk, Dh)),
            "v_cache": _w((lambda *s: torch.randn(*s, dtype=torch.bfloat16) * 0.2)(B, Hkv, Sk, Dh)),
            "wq": _w((lambda *s: torch.randn(*s, dtype=torch.bfloat16) * 1.6)(D, Hq * Dh) * (D ** -0.5)),
            "wo": _w((lambda *s: torch.randn(*s, dtype=torch.bfloat16) * 0.2)(Hq * Dh, D) * ((Hq * Dh) ** -0.5)),
        }
    _B_in = _B_setup()

    return {
        "A_x": _A_in["x"],
        "A_k_cache": _A_in["k_cache"],
        "A_v_cache": _A_in["v_cache"],
        "A_wq": _A_in["wq"],
        "A_wo": _A_in["wo"],
        "B_x": _B_in["x"],
        "B_k_cache": _B_in["k_cache"],
        "B_v_cache": _B_in["v_cache"],
        "B_wq": _B_in["wq"],
        "B_wo": _B_in["wo"],
    }

def reference(A_x, A_k_cache, A_v_cache, A_wq, A_wo, B_x, B_k_cache, B_v_cache, B_wq, B_wo):
    def _A_ref(x, k_cache, v_cache, wq, wo):
        B, Sq, D = x.shape
        H, Dh = 32, 128
        q = (lambda x, *d: x.permute(*d))((x @ wq).reshape(B, Sq, H, Dh), 0, 2, 1, 3)
        scores = torch.einsum("bhqd,bhkd->bhqk", q, k_cache) * (Dh ** -0.5)
        p = _softmax(scores, axis=-1)
        o = torch.einsum("bhqk,bhkd->bhqd", p, v_cache)
        return (lambda x, *d: x.permute(*d))(o, 0, 2, 1, 3).reshape(B, Sq, H * Dh) @ wo

    def _B_ref(x, k_cache, v_cache, wq, wo):
        B, Sq, D = x.shape
        Hq, Dh = 32, 128
        Hkv = k_cache.shape[1]
        rep = Hq // Hkv
        q = (lambda x, *d: x.permute(*d))((x @ wq).reshape(B, Sq, Hq, Dh), 0, 2, 1, 3)
        k = (lambda x, n, axis: torch.repeat_interleave(x, n, dim=axis))(k_cache, rep, axis=1)
        v = (lambda x, n, axis: torch.repeat_interleave(x, n, dim=axis))(v_cache, rep, axis=1)
        scores = torch.einsum("bhqd,bhkd->bhqk", q, k) * (Dh ** -0.5)
        p = _softmax(scores, axis=-1)
        o = torch.einsum("bhqk,bhkd->bhqd", p, v)
        return (lambda x, *d: x.permute(*d))(o, 0, 2, 1, 3).reshape(B, Sq, Hq * Dh) @ wo

    a = _A_ref(A_x, A_k_cache, A_v_cache, A_wq, A_wo)
    b = _B_ref(B_x, B_k_cache, B_v_cache, B_wq, B_wo)
    return torch.concatenate([a, b], axis=-1)
```

```cpp
#include <hip/hip_runtime.h>
#include <hip/hip_bf16.h>
#include <utility>

#define RING 32
#define KV_AUX 2

typedef unsigned v4u __attribute__((ext_vector_type(4)));
typedef float f32x4 __attribute__((ext_vector_type(4)));
typedef float f32x2 __attribute__((ext_vector_type(2)));
typedef short bf16x8 __attribute__((ext_vector_type(8)));
typedef __amdgpu_buffer_rsrc_t rsrc_t;

constexpr int DM = 640, NH = 32, SK = 8192, DH = 128;
constexpr int NSPLIT = 4, NW = 8, NTHR = NW * 64;
constexpr int KEYS_WG = SK / NSPLIT, KEYS_W = KEYS_WG / NW;
constexpr int NT = KEYS_W / 16;
constexpr int NQL = DM / NW / 4;
constexpr int NKL = NT * 4;
constexpr int NOL = (DH / NW / 4) * 5;
constexpr int I_K = NQL, I_V = I_K + NKL, I_O = I_V + NKL, I_END = I_O + NOL;
constexpr int W = RING;
constexpr float C2 = 0.088388347648318440f * 1.4426950408889634f;
constexpr int PSTRIDE = 656;

struct Params {
  const void* x[2]; const void* k[2]; const void* v[2]; const void* wq[2]; const void* wo[2];
  float* ws; unsigned short* out;
};

template <int N, typename F, int... Is>
__device__ __forceinline__ void sfor_impl(F&& f, std::integer_sequence<int, Is...>) { (f(std::integral_constant<int, Is>{}), ...); }
template <int N, typename F> __device__ __forceinline__ void sfor(F&& f) { sfor_impl<N>(f, std::make_integer_sequence<int, N>{}); }

__device__ __forceinline__ rsrc_t mkrsrc(const void* p, int bytes) {
  return __builtin_amdgcn_make_buffer_rsrc(const_cast<void*>(p), 0, bytes, 0x00020000);
}
__device__ __forceinline__ f32x4 mfma(v4u a, v4u b, f32x4 c) {
  return __builtin_amdgcn_mfma_f32_16x16x32_bf16(__builtin_bit_cast(bf16x8, a), __builtin_bit_cast(bf16x8, b), c, 0, 0, 0);
}
template <int CTRL> __device__ __forceinline__ unsigned dppu(unsigned x) {
  return (unsigned)__builtin_amdgcn_mov_dpp((int)x, CTRL, 0xf, 0xf, true);
}
template <int CTRL> __device__ __forceinline__ float dppf(float x) {
  return __builtin_bit_cast(float, __builtin_amdgcn_mov_dpp(__builtin_bit_cast(int, x), CTRL, 0xf, 0xf, true));
}
__device__ __forceinline__ unsigned cvtpk(float lo, float hi) {
  typedef __bf16 bf2 __attribute__((ext_vector_type(2)));
  bf2 r; r[0] = (__bf16)lo; r[1] = (__bf16)hi;
  return __builtin_bit_cast(unsigned, r);
}
__device__ __forceinline__ float xrow_max(float x) {
  auto s = __builtin_amdgcn_permlane16_swap(__float_as_uint(x), __float_as_uint(x), false, false);
  x = fmaxf(__uint_as_float(s[0]), __uint_as_float(s[1]));
  auto t = __builtin_amdgcn_permlane32_swap(__float_as_uint(x), __float_as_uint(x), false, false);
  return fmaxf(__uint_as_float(t[0]), __uint_as_float(t[1]));
}
__device__ __forceinline__ float xrow_sum(float x) {
  auto s = __builtin_amdgcn_permlane16_swap(__float_as_uint(x), __float_as_uint(x), false, false);
  x = __uint_as_float(s[0]) + __uint_as_float(s[1]);
  auto t = __builtin_amdgcn_permlane32_swap(__float_as_uint(x), __float_as_uint(x), false, false);
  return __uint_as_float(t[0]) + __uint_as_float(t[1]);
}
constexpr int ROR4 = 0x124, ROR8 = 0x128, BCAST = 0x150;

constexpr int L_QP = 0;
constexpr int L_QB = L_QP + NW * 128;
constexpr int L_ACC = L_QB + NW * 64;
constexpr int L_ML = L_ACC + NW * 128;
constexpr int L_Y = L_ML + 2 * NW;
constexpr int L_END = L_Y + 2 * NW * DM;

__global__ void __launch_bounds__(NTHR, 2) attn_main(Params p) {
  __shared__ __attribute__((aligned(16))) float lds[L_END];
  const int b = blockIdx.x;
  const int hh = (b & 7) * 8 + (b >> 5), s = (b >> 3) & 3;
  const int br = hh >> 5, h = hh & 31;
  const int tid = threadIdx.x, lane = tid & 63, w = __builtin_amdgcn_readfirstlane(tid >> 6);
  const int g = lane >> 4, c = lane & 15;

  const size_t kvoff = ((size_t)h * SK + s * KEYS_WG + w * KEYS_W) * (DH * 2);
  const rsrc_t rx = mkrsrc(p.x[br], DM * 2);
  const rsrc_t rq = mkrsrc((const char*)p.wq[br] + (size_t)w * (4 * NQL) * (NH * DH * 2) + h * (DH * 2), (4 * NQL - 1) * (NH * DH * 2) + DH * 2);
  const rsrc_t rk = mkrsrc((const char*)p.k[br] + kvoff, KEYS_W * DH * 2);
  const rsrc_t rv = mkrsrc((const char*)p.v[br] + kvoff, KEYS_W * DH * 2);
  const rsrc_t ro = mkrsrc((const char*)p.wo[br] + (size_t)(h * DH + w * (DH / NW)) * (DM * 2), (DH / NW) * DM * 2);

  const int xo = (w * (4 * NQL) + g * NQL) * 2;
  unsigned x0 = __builtin_amdgcn_raw_buffer_load_b16(rx, xo + c * 2, 0, 0);
  unsigned x1 = __builtin_amdgcn_raw_buffer_load_b16(rx, xo + 32 + (c & 3) * 2, 0, 0);

  const int q_lo = g * NQL * (NH * DH * 2) + c * 16;
  const int k_lo = (c >> 2) * 256 + g * 64 + (c & 3) * 16;
  const int v_lo = g * 1024 + c * 16;
  const int o_lo = g * (DM * 2) + c * 16;

  v4u ring[W];
  auto issue = [&](auto N) {
    constexpr int n = N;
    if constexpr (n < I_K) ring[n % W] = __builtin_amdgcn_raw_buffer_load_b128(rq, q_lo, n * (NH * DH * 2), 0);
    else if constexpr (n < I_V) { constexpr int i = n - I_K; ring[n % W] = __builtin_amdgcn_raw_buffer_load_b128(rk, k_lo + (i & 3) * 1024, (i >> 2) * 4096, KV_AUX); }
    else if constexpr (n < I_O) { constexpr int i = n - I_V; ring[n % W] = __builtin_amdgcn_raw_buffer_load_b128(rv, v_lo + (i & 3) * 256, (i >> 2) * 4096, KV_AUX); }
    else if constexpr (n < I_END) { constexpr int i = n - I_O; ring[n % W] = __builtin_amdgcn_raw_buffer_load_b128(ro, o_lo + (i % 5) * 256, (i / 5) * 4 * (DM * 2), 0); }
  };
  sfor<W>([&](auto N) { issue(N); });
  __builtin_amdgcn_sched_barrier(0);

  unsigned mask[4];
#pragma unroll
  for (int k = 0; k < 4; ++k) mask[k] = (c < 8 && (c >> 1) == k) ? ((c & 1) ? 0xffff0000u : 0x0000ffffu) : 0u;

  x0 |= x0 << 16; x1 |= x1 << 16;
  f32x4 qa = {0.f, 0.f, 0.f, 0.f};
  sfor<NQL>([&](auto N) {
    constexpr int n = N;
    const unsigned xb = n < 16 ? dppu<BCAST + (n & 15)>(x0) : dppu<BCAST + (n & 15)>(x1);
    const v4u a = {xb & mask[0], xb & mask[1], xb & mask[2], xb & mask[3]};
    qa = mfma(a, ring[n % W], qa);
    issue(std::integral_constant<int, n + W>{});
    __builtin_amdgcn_sched_barrier(0);
  });
  if (g < 2) *reinterpret_cast<f32x4*>(&lds[L_QP + w * 128 + 8 * c + 4 * g]) = qa;
  __syncthreads();
  v4u aq[4];
  {
    f32x2 t = {0.f, 0.f};
#pragma unroll
    for (int ww = 0; ww < NW; ++ww) t += *reinterpret_cast<const f32x2*>(&lds[L_QP + ww * 128 + 2 * lane]);
    unsigned* qb = reinterpret_cast<unsigned*>(&lds[L_QB + w * 64]);
    qb[lane] = cvtpk(t[0], t[1]);
    __builtin_amdgcn_fence(__ATOMIC_RELEASE, "wavefront");
    __builtin_amdgcn_wave_barrier();
    __builtin_amdgcn_fence(__ATOMIC_ACQUIRE, "wavefront");
    const v4u qf = *reinterpret_cast<const v4u*>(qb + 4 * (4 * g + (c & 3)));
#pragma unroll
    for (int i = 0; i < 4; ++i) aq[i] = ((c >> 2) == i) ? qf : v4u{0u, 0u, 0u, 0u};
  }
  __builtin_amdgcn_sched_barrier(0);

  float sc[NT];
  sfor<NT>([&](auto T) {
    constexpr int t = T;
    f32x4 d = {0.f, 0.f, 0.f, 0.f};
    sfor<4>([&](auto I) {
      constexpr int n = I_K + t * 4 + I;
      d = mfma(aq[I], ring[n % W], d);
      issue(std::integral_constant<int, n + W>{});
      __builtin_amdgcn_sched_barrier(0);
    });
    sc[t] = (dppf<0x00>(d[0]) + dppf<0x55>(d[1])) + (dppf<0xAA>(d[2]) + dppf<0xFF>(d[3]));
  });
  float m = sc[0];
#pragma unroll
  for (int t = 1; t < NT; ++t) m = fmaxf(m, sc[t]);
  m = fmaxf(m, dppf<ROR4>(m)); m = fmaxf(m, dppf<ROR8>(m)); m = xrow_max(m);
  const float mc = m * C2;
  float l = 0.f;
  unsigned pp[NT];
#pragma unroll
  for (int t = 0; t < NT; ++t) { const float e = __builtin_amdgcn_exp2f(fmaf(sc[t], C2, -mc)); l += e; pp[t] = cvtpk(e, e); }
  l += dppf<ROR4>(l); l += dppf<ROR8>(l); l = xrow_sum(l);
  __builtin_amdgcn_sched_barrier(0);

  f32x4 acc = {0.f, 0.f, 0.f, 0.f};
  sfor<NKL>([&](auto I) {
    constexpr int i = I, n = I_V + i;
    const unsigned pb = dppu<BCAST + 4 * (i & 3)>(pp[i >> 2]);
    const v4u a = {pb & mask[0], pb & mask[1], pb & mask[2], pb & mask[3]};
    acc = mfma(a, ring[n % W], acc);
    issue(std::integral_constant<int, n + W>{});
    __builtin_amdgcn_sched_barrier(0);
  });
  if (g < 2) *reinterpret_cast<f32x4*>(&lds[L_ACC + w * 128 + 8 * c + 4 * g]) = acc;
  if (lane == 0) *reinterpret_cast<f32x2*>(&lds[L_ML + 2 * w]) = f32x2{mc, l};
  __syncthreads();

  float M = -INFINITY, L = 0.f, o = 0.f;
  {
    f32x2 ml[NW];
#pragma unroll
    for (int ww = 0; ww < NW; ++ww) { ml[ww] = *reinterpret_cast<const f32x2*>(&lds[L_ML + 2 * ww]); M = fmaxf(M, ml[ww][0]); }
    const int row = w * (DH / NW) + 4 * (c & 3) + g;
#pragma unroll
    for (int ww = 0; ww < NW; ++ww) { const float wt = __builtin_amdgcn_exp2f(ml[ww][0] - M); L = fmaf(wt, ml[ww][1], L); o = fmaf(wt, lds[L_ACC + ww * 128 + row], o); }
  }
  const unsigned ohi = cvtpk(o, o);
  const float ores = o - __uint_as_float(ohi & 0xffff0000u);
  const unsigned olo = cvtpk(ores, ores);
  unsigned mask2[4];
#pragma unroll
  for (int k = 0; k < 4; ++k) mask2[k] = (((c & 7) >> 1) == k) ? ((c & 1) ? 0xffff0000u : 0x0000ffffu) : 0u;
  f32x4 y[5];
#pragma unroll
  for (int cg = 0; cg < 5; ++cg) y[cg] = f32x4{0.f, 0.f, 0.f, 0.f};
  sfor<DH / NW / 4>([&](auto RG) {
    constexpr int rg = RG;
    const unsigned hb = dppu<BCAST + rg>(ohi), lb = dppu<BCAST + rg>(olo);
    const unsigned ob = c < 8 ? hb : lb;
    const v4u a = {ob & mask2[0], ob & mask2[1], ob & mask2[2], ob & mask2[3]};
    sfor<5>([&](auto CG) { constexpr int n = I_O + rg * 5 + CG; y[CG] = mfma(a, ring[n % W], y[CG]); });
  });
#pragma unroll
  for (int cg = 0; cg < 5; ++cg) *reinterpret_cast<f32x4*>(&lds[L_Y + (2 * w + (g >> 1)) * DM + cg * 128 + 8 * c + 4 * (g & 1)]) = y[cg];
  __syncthreads();
  float* rec = p.ws + (size_t)(hh * NSPLIT + s) * PSTRIDE;
  if (tid < DM / 2) {
    f32x2 t = {0.f, 0.f};
#pragma unroll
    for (int j = 0; j < 2 * NW; ++j) t += *reinterpret_cast<const f32x2*>(&lds[L_Y + j * DM + 2 * tid]);
    *reinterpret_cast<f32x2*>(rec + 2 * tid) = t;
  } else if (tid == DM / 2) {
    *reinterpret_cast<f32x2*>(rec + DM) = f32x2{M, L};
  }
}

__global__ void __launch_bounds__(512) attn_reduce(const float* __restrict__ ws, unsigned short* __restrict__ out) {
  __shared__ __attribute__((aligned(16))) float red[8][16];
  const int br = blockIdx.y, c0 = blockIdx.x * 16;
  const int tid = threadIdx.x, lane = tid & 63, w = tid >> 6;
  const int part = tid >> 2, q = tid & 3;
  const float* base = ws + (size_t)br * (NH * NSPLIT) * PSTRIDE;
  const f32x4 yv = *reinterpret_cast<const f32x4*>(base + (size_t)part * PSTRIDE + c0 + 4 * q);
  const f32x2 ml = *reinterpret_cast<const f32x2*>(base + (size_t)part * PSTRIDE + DM);
  float M = ml[0]; M = fmaxf(M, dppf<ROR4>(M)); M = fmaxf(M, dppf<ROR8>(M));
  const float wt = __builtin_amdgcn_exp2f(ml[0] - M);
  float L = wt * ml[1]; L += dppf<ROR4>(L); L += dppf<ROR8>(L);
  const float f = wt / L;
  f32x4 r;
#pragma unroll
  for (int j = 0; j < 4; ++j) { float t = yv[j] * f; t += dppf<ROR4>(t); t += dppf<ROR8>(t); r[j] = xrow_sum(t); }
  if (lane < 4) *reinterpret_cast<f32x4*>(&red[w][4 * lane]) = r;
  __syncthreads();
  if (tid < 8) {
    float a = 0.f, bb = 0.f;
#pragma unroll
    for (int j = 0; j < 8; ++j) { a += red[j][2 * tid]; bb += red[j][2 * tid + 1]; }
    *reinterpret_cast<unsigned*>(out + br * DM + c0 + 2 * tid) = cvtpk(a, bb);
  }
}

extern "C" void kernel_launch(void* const* d_in, const int* in_sizes, int n_in,
                              void* d_out, int out_size, void* d_ws, size_t ws_size,
                              hipStream_t stream) {
  Params p;
  for (int i = 0; i < 2; ++i) {
    p.x[i] = d_in[5 * i + 0]; p.k[i] = d_in[5 * i + 1]; p.v[i] = d_in[5 * i + 2]; p.wq[i] = d_in[5 * i + 3]; p.wo[i] = d_in[5 * i + 4];
  }
  p.ws = (float*)d_ws; p.out = (unsigned short*)d_out;
  hipLaunchKernelGGL(attn_main, dim3(256), dim3(NTHR), 0, stream, p);
  hipLaunchKernelGGL(attn_reduce, dim3(DM / 16, 2), dim3(512), 0, stream, (const float*)d_ws, (unsigned short*)d_out);
}
```

```cpp
#define PAT 3
#define QBAR 1
#define WQ_AUX 2
#define WO_AUX 2
#define RING 24
#include <hip/hip_runtime.h>
#include <hip/hip_bf16.h>
#include <utility>
#include <cstdio>

#define KV_AUX 2

typedef unsigned v4u __attribute__((ext_vector_type(4)));
typedef float f32x4 __attribute__((ext_vector_type(4)));
typedef float f32x2 __attribute__((ext_vector_type(2)));
typedef short bf16x8 __attribute__((ext_vector_type(8)));
typedef __amdgpu_buffer_rsrc_t rsrc_t;

constexpr int DM = 640, NH = 32, SK = 8192, DH = 128;
constexpr int NSPLIT = 4, NW = 8, NTHR = NW * 64;
constexpr int KEYS_WG = SK / NSPLIT, KEYS_W = KEYS_WG / NW;
constexpr int NT = KEYS_W / 16;
constexpr int NQL = DM / NW / 4;
constexpr int NKL = NT * 4;
constexpr int NOL = (DH / NW / 4) * 5;
constexpr int I_K = NQL, I_V = I_K + NKL, I_O = I_V + NKL, I_END = I_O + NOL;
constexpr int W = RING;
constexpr float C2 = 0.088388347648318440f * 1.4426950408889634f;
constexpr int PSTRIDE = 656;

struct Params {
  const void* x[2]; const void* k[2]; const void* v[2]; const void* wq[2]; const void* wo[2];
  float* ws; unsigned short* out;
};

template <int N, typename F, int... Is>
__device__ __forceinline__ void sfor_impl(F&& f, std::integer_sequence<int, Is...>) { (f(std::integral_constant<int, Is>{}), ...); }
template <int N, typename F> __device__ __forceinline__ void sfor(F&& f) { sfor_impl<N>(f, std::make_integer_sequence<int, N>{}); }

__device__ __forceinline__ rsrc_t mkrsrc(const void* p, int bytes) {
  return __builtin_amdgcn_make_buffer_rsrc(const_cast<void*>(p), 0, bytes, 0x00020000);
}
__device__ __forceinline__ f32x4 mfma(v4u a, v4u b, f32x4 c) {
  return __builtin_amdgcn_mfma_f32_16x16x32_bf16(__builtin_bit_cast(bf16x8, a), __builtin_bit_cast(bf16x8, b), c, 0, 0, 0);
}
template <int CTRL> __device__ __forceinline__ unsigned dppu(unsigned x) {
  return (unsigned)__builtin_amdgcn_mov_dpp((int)x, CTRL, 0xf, 0xf, true);
}
template <int CTRL> __device__ __forceinline__ float dppf(float x) {
  return __builtin_bit_cast(float, __builtin_amdgcn_mov_dpp(__builtin_bit_cast(int, x), CTRL, 0xf, 0xf, true));
}
__device__ __forceinline__ unsigned cvtpk(float lo, float hi) {
  typedef __bf16 bf2 __attribute__((ext_vector_type(2)));
  bf2 r; r[0] = (__bf16)lo; r[1] = (__bf16)hi;
  return __builtin_bit_cast(unsigned, r);
}
__device__ __forceinline__ float xrow_max(float x) {
  auto s = __builtin_amdgcn_permlane16_swap(__float_as_uint(x), __float_as_uint(x), false, false);
  x = fmaxf(__uint_as_float(s[0]), __uint_as_float(s[1]));
  auto t = __builtin_amdgcn_permlane32_swap(__float_as_uint(x), __float_as_uint(x), false, false);
  return fmaxf(__uint_as_float(t[0]), __uint_as_float(t[1]));
}
__device__ __forceinline__ float xrow_sum(float x) {
  auto s = __builtin_amdgcn_permlane16_swap(__float_as_uint(x), __float_as_uint(x), false, false);
  x = __uint_as_float(s[0]) + __uint_as_float(s[1]);
  auto t = __builtin_amdgcn_permlane32_swap(__float_as_uint(x), __float_as_uint(x), false, false);
  return __uint_as_float(t[0]) + __uint_as_float(t[1]);
}
#define STAMP(k) do { } while (0)
constexpr int ROR4 = 0x124, ROR8 = 0x128, BCAST = 0x150;

constexpr int L_QP = 0;
constexpr int L_QB = L_QP + NW * 128;
constexpr int L_ACC = L_QB + NW * 64;
constexpr int L_ML = L_ACC + NW * 128;
constexpr int L_Y = L_ML + 2 * NW;
constexpr int L_END = L_Y + 2 * NW * DM;

__global__ void __launch_bounds__(NTHR, 2) attn_main(Params p) {
  __shared__ __attribute__((aligned(16))) float lds[L_END];
  STAMP(0);
  const int b = blockIdx.x;
  const int hh = (b & 7) * 8 + (b >> 5), s = (b >> 3) & 3;
  const int br = hh >> 5, h = hh & 31;
  const int tid = threadIdx.x, lane = tid & 63, w = __builtin_amdgcn_readfirstlane(tid >> 6);
  const int g = lane >> 4, c = lane & 15;

  const size_t kvoff = (size_t)h * SK * (DH * 2);
  const int rot = PAT == 0 ? 0 : (int)((unsigned)(hh + 1) * 2654435761u >> 20);
  auto tile_off = [&](int k) {
    const int j = (k + rot) & 15;
    int t;
    if (PAT == 0) t = j | (w << 4) | (s << 7);
    else if (PAT == 1) t = w | (s << 3) | (j << 5);
    else if (PAT == 2) t = (j & 1) | ((w & 1) << 1) | ((s & 1) << 2) | ((w & 2) << 2) | ((j >> 1) << 4) | ((s >> 1) << 7) | ((w >> 2) << 8);
    else t = w | ((j & 1) << 3) | ((s & 1) << 4) | (((j >> 1) & 3) << 5) | ((s >> 1) << 7) | ((j >> 3) << 8);
    return t * 4096;
  };
  const rsrc_t rx = mkrsrc(p.x[br], DM * 2);
  const rsrc_t rq = mkrsrc((const char*)p.wq[br] + (size_t)w * (4 * NQL) * (NH * DH * 2) + h * (DH * 2), (4 * NQL - 1) * (NH * DH * 2) + DH * 2);
  const rsrc_t rk = mkrsrc((const char*)p.k[br] + kvoff, SK * DH * 2);
  const rsrc_t rv = mkrsrc((const char*)p.v[br] + kvoff, SK * DH * 2);
  const rsrc_t ro = mkrsrc((const char*)p.wo[br] + (size_t)(h * DH + w * (DH / NW)) * (DM * 2), (DH / NW) * DM * 2);

  const int xo = (w * (4 * NQL) + g * NQL) * 2;
  unsigned x0 = __builtin_amdgcn_raw_buffer_load_b16(rx, xo + c * 2, 0, 0);
  unsigned x1 = __builtin_amdgcn_raw_buffer_load_b16(rx, xo + 32 + (c & 3) * 2, 0, 0);

  const int q_lo = g * NQL * (NH * DH * 2) + c * 16;
  const int k_lo = (c >> 2) * 256 + g * 64 + (c & 3) * 16;
  const int v_lo = g * 1024 + c * 16;
  const int o_lo = g * (DM * 2) + c * 16;

  v4u ring[W];
  auto issue = [&](auto N) {
    constexpr int n = N;
    if constexpr (n < I_K) ring[n % W] = __builtin_amdgcn_raw_buffer_load_b128(rq, q_lo, n * (NH * DH * 2), WQ_AUX);
    else if constexpr (n < I_V) { constexpr int i = n - I_K; ring[n % W] = __builtin_amdgcn_raw_buffer_load_b128(rk, k_lo + (i & 3) * 1024, tile_off(i >> 2), KV_AUX); }
    else if constexpr (n < I_O) { constexpr int i = n - I_V; ring[n % W] = __builtin_amdgcn_raw_buffer_load_b128(rv, v_lo + (i & 3) * 256, tile_off(i >> 2), KV_AUX); }
    else if constexpr (n < I_END) { constexpr int i = n - I_O; ring[n % W] = __builtin_amdgcn_raw_buffer_load_b128(ro, o_lo + (i % 5) * 256, (i / 5) * 4 * (DM * 2), WO_AUX); }
  };
  sfor<W>([&](auto N) {
    if constexpr (QBAR && N == NQL) { __builtin_amdgcn_sched_barrier(0); __builtin_amdgcn_s_barrier(); __builtin_amdgcn_sched_barrier(0); }
    issue(N); });
  __builtin_amdgcn_sched_barrier(0);
  STAMP(1);

  unsigned mask[4];
#pragma unroll
  for (int k = 0; k < 4; ++k) mask[k] = (c < 8 && (c >> 1) == k) ? ((c & 1) ? 0xffff0000u : 0x0000ffffu) : 0u;

  x0 |= x0 << 16; x1 |= x1 << 16;
  f32x4 qa = {0.f, 0.f, 0.f, 0.f};
  sfor<NQL>([&](auto N) {
    constexpr int n = N;
    const unsigned xb = n < 16 ? dppu<BCAST + (n & 15)>(x0) : dppu<BCAST + (n & 15)>(x1);
    const v4u a = {xb & mask[0], xb & mask[1], xb & mask[2], xb & mask[3]};
    qa = mfma(a, ring[n % W], qa);
    issue(std::integral_constant<int, n + W>{});
    __builtin_amdgcn_sched_barrier(0);
  });
  STAMP(2);
  if (g < 2) *reinterpret_cast<f32x4*>(&lds[L_QP + w * 128 + 8 * c + 4 * g]) = qa;
  __syncthreads();
  v4u aq[4];
  {
    f32x2 t = {0.f, 0.f};
#pragma unroll
    for (int ww = 0; ww < NW; ++ww) t += *reinterpret_cast<const f32x2*>(&lds[L_QP + ww * 128 + 2 * lane]);
    unsigned* qb = reinterpret_cast<unsigned*>(&lds[L_QB + w * 64]);
    qb[lane] = cvtpk(t[0], t[1]);
    __builtin_amdgcn_fence(__ATOMIC_RELEASE, "wavefront");
    __builtin_amdgcn_wave_barrier();
    __builtin_amdgcn_fence(__ATOMIC_ACQUIRE, "wavefront");
    const v4u qf = *reinterpret_cast<const v4u*>(qb + 4 * (4 * g + (c & 3)));
#pragma unroll
    for (int i = 0; i < 4; ++i) aq[i] = ((c >> 2) == i) ? qf : v4u{0u, 0u, 0u, 0u};
  }
  __builtin_amdgcn_sched_barrier(0);

  STAMP(3);
  float sc[NT];
  sfor<NT>([&](auto T) {
    constexpr int t = T;
    f32x4 d = {0.f, 0.f, 0.f, 0.f};
    sfor<4>([&](auto I) {
      constexpr int n = I_K + t * 4 + I;
      d = mfma(aq[I], ring[n % W], d);
      issue(std::integral_constant<int, n + W>{});
      __builtin_amdgcn_sched_barrier(0);
    });
    sc[t] = (dppf<0x00>(d[0]) + dppf<0x55>(d[1])) + (dppf<0xAA>(d[2]) + dppf<0xFF>(d[3]));
  });
  float m = sc[0];
#pragma unroll
  for (int t = 1; t < NT; ++t) m = fmaxf(m, sc[t]);
  m = fmaxf(m, dppf<ROR4>(m)); m = fmaxf(m, dppf<ROR8>(m)); m = xrow_max(m);
  const float mc = m * C2;
  float l = 0.f;
  unsigned pp[NT];
#pragma unroll
  for (int t = 0; t < NT; ++t) { const float e = __builtin_amdgcn_exp2f(fmaf(sc[t], C2, -mc)); l += e; pp[t] = cvtpk(e, e); }
  l += dppf<ROR4>(l); l += dppf<ROR8>(l); l = xrow_sum(l);
  __builtin_amdgcn_sched_barrier(0);

  STAMP(4);
  f32x4 acc = {0.f, 0.f, 0.f, 0.f};
  sfor<NKL>([&](auto I) {
    constexpr int i = I, n = I_V + i;
    const unsigned pb = dppu<BCAST + 4 * (i & 3)>(pp[i >> 2]);
    const v4u a = {pb & mask[0], pb & mask[1], pb & mask[2], pb & mask[3]};
    acc = mfma(a, ring[n % W], acc);
    issue(std::integral_constant<int, n + W>{});
    __builtin_amdgcn_sched_barrier(0);
  });
  STAMP(5);
  if (g < 2) *reinterpret_cast<f32x4*>(&lds[L_ACC + w * 128 + 8 * c + 4 * g]) = acc;
  if (lane == 0) *reinterpret_cast<f32x2*>(&lds[L_ML + 2 * w]) = f32x2{mc, l};
  __syncthreads();

  STAMP(6);
  float M = -INFINITY, L = 0.f, o = 0.f;
  {
    f32x2 ml[NW];
#pragma unroll
    for (int ww = 0; ww < NW; ++ww) { ml[ww] = *reinterpret_cast<const f32x2*>(&lds[L_ML + 2 * ww]); M = fmaxf(M, ml[ww][0]); }
    const int row = w * (DH / NW) + 4 * (c & 3) + g;
#pragma unroll
    for (int ww = 0; ww < NW; ++ww) { const float wt = __builtin_amdgcn_exp2f(ml[ww][0] - M); L = fmaf(wt, ml[ww][1], L); o = fmaf(wt, lds[L_ACC + ww * 128 + row], o); }
  }
  const unsigned ohi = cvtpk(o, o);
  const float ores = o - __uint_as_float(ohi & 0xffff0000u);
  const unsigned olo = cvtpk(ores, ores);
  unsigned mask2[4];
#pragma unroll
  for (int k = 0; k < 4; ++k) mask2[k] = (((c & 7) >> 1) == k) ? ((c & 1) ? 0xffff0000u : 0x0000ffffu) : 0u;
  f32x4 y[5];
#pragma unroll
  for (int cg = 0; cg < 5; ++cg) y[cg] = f32x4{0.f, 0.f, 0.f, 0.f};
  sfor<DH / NW / 4>([&](auto RG) {
    constexpr int rg = RG;
    const unsigned hb = dppu<BCAST + rg>(ohi), lb = dppu<BCAST + rg>(olo);
    const unsigned ob = c < 8 ? hb : lb;
    const v4u a = {ob & mask2[0], ob & mask2[1], ob & mask2[2], ob & mask2[3]};
    sfor<5>([&](auto CG) { constexpr int n = I_O + rg * 5 + CG; y[CG] = mfma(a, ring[n % W], y[CG]); });
  });
#pragma unroll
  for (int cg = 0; cg < 5; ++cg) *reinterpret_cast<f32x4*>(&lds[L_Y + (2 * w + (g >> 1)) * DM + cg * 128 + 8 * c + 4 * (g & 1)]) = y[cg];
  __syncthreads();
  float* rec = p.ws + (size_t)(hh * NSPLIT + s) * PSTRIDE;
  if (tid < DM / 2) {
    f32x2 t = {0.f, 0.f};
#pragma unroll
    for (int j = 0; j < 2 * NW; ++j) t += *reinterpret_cast<const f32x2*>(&lds[L_Y + j * DM + 2 * tid]);
    *reinterpret_cast<f32x2*>(rec + 2 * tid) = t;
  } else if (tid == DM / 2) {
    *reinterpret_cast<f32x2*>(rec + DM) = f32x2{M, L};
  }
}

__global__ void __launch_bounds__(512) attn_reduce(const float* __restrict__ ws, unsigned short* __restrict__ out) {
  __shared__ __attribute__((aligned(16))) float red[8][16];
  const int br = blockIdx.y, c0 = blockIdx.x * 16;
  const int tid = threadIdx.x, lane = tid & 63, w = tid >> 6;
  const int part = tid >> 2, q = tid & 3;
  const float* base = ws + (size_t)br * (NH * NSPLIT) * PSTRIDE;
  const f32x4 yv = *reinterpret_cast<const f32x4*>(base + (size_t)part * PSTRIDE + c0 + 4 * q);
  const f32x2 ml = *reinterpret_cast<const f32x2*>(base + (size_t)part * PSTRIDE + DM);
  float M = ml[0]; M = fmaxf(M, dppf<ROR4>(M)); M = fmaxf(M, dppf<ROR8>(M));
  const float wt = __builtin_amdgcn_exp2f(ml[0] - M);
  float L = wt * ml[1]; L += dppf<ROR4>(L); L += dppf<ROR8>(L);
  const float f = wt / L;
  f32x4 r;
#pragma unroll
  for (int j = 0; j < 4; ++j) { float t = yv[j] * f; t += dppf<ROR4>(t); t += dppf<ROR8>(t); r[j] = xrow_sum(t); }
  if (lane < 4) *reinterpret_cast<f32x4*>(&red[w][4 * lane]) = r;
  __syncthreads();
  if (tid < 8) {
    float a = 0.f, bb = 0.f;
#pragma unroll
    for (int j = 0; j < 8; ++j) { a += red[j][2 * tid]; bb += red[j][2 * tid + 1]; }
    *reinterpret_cast<unsigned*>(out + br * DM + c0 + 2 * tid) = cvtpk(a, bb);
  }
}

extern "C" void kernel_launch(void* const* d_in, const int* in_sizes, int n_in,
                              void* d_out, int out_size, void* d_ws, size_t ws_size,
                              hipStream_t stream) {
  Params p;
  for (int i = 0; i < 2; ++i) {
    p.x[i] = d_in[5 * i + 0]; p.k[i] = d_in[5 * i + 1]; p.v[i] = d_in[5 * i + 2]; p.wq[i] = d_in[5 * i + 3]; p.wo[i] = d_in[5 * i + 4];
  }
  for (int i = 0; i < 10; ++i) fprintf(stderr, "in%d %p ", i, d_in[i]); fprintf(stderr, "out %p ws %p\n", d_out, d_ws);
  p.ws = (float*)d_ws; p.out = (unsigned short*)d_out;
  hipLaunchKernelGGL(attn_main, dim3(256), dim3(NTHR), 0, stream, p);
  hipLaunchKernelGGL(attn_reduce, dim3(DM / 16, 2), dim3(512), 0, stream, (const float*)d_ws, (unsigned short*)d_out);
}
```

```cpp
#define PAT 3
#define QBAR 1
#define RING 32
#define AK 0
#define AV 0
#define AQ 2
#define AO 2
#define BK 2
#define BV 2
#define BQ 2
#define BO 2
#include <hip/hip_runtime.h>
#include <hip/hip_bf16.h>
#include <utility>

#define KV_AUX 2
#define WQ_AUX 0
#define WO_AUX 0

typedef unsigned v4u __attribute__((ext_vector_type(4)));
typedef float f32x4 __attribute__((ext_vector_type(4)));
typedef float f32x2 __attribute__((ext_vector_type(2)));
typedef short bf16x8 __attribute__((ext_vector_type(8)));
typedef __amdgpu_buffer_rsrc_t rsrc_t;

constexpr int DM = 640, NH = 32, SK = 8192, DH = 128;
constexpr int NSPLIT = 4, NW = 8, NTHR = NW * 64;
constexpr int KEYS_WG = SK / NSPLIT, KEYS_W = KEYS_WG / NW;
constexpr int NT = KEYS_W / 16;
constexpr int NQL = DM / NW / 4;
constexpr int NKL = NT * 4;
constexpr int NOL = (DH / NW / 4) * 5;
constexpr int I_K = NQL, I_V = I_K + NKL, I_O = I_V + NKL, I_END = I_O + NOL;
constexpr int W = RING;
constexpr float C2 = 0.088388347648318440f * 1.4426950408889634f;
constexpr int PSTRIDE = 656;

struct Params {
  const void* x[2]; const void* k[2]; const void* v[2]; const void* wq[2]; const void* wo[2];
  float* ws; unsigned short* out;
};

template <int N, typename F, int... Is>
__device__ __forceinline__ void sfor_impl(F&& f, std::integer_sequence<int, Is...>) { (f(std::integral_constant<int, Is>{}), ...); }
template <int N, typename F> __device__ __forceinline__ void sfor(F&& f) { sfor_impl<N>(f, std::make_integer_sequence<int, N>{}); }

__device__ __forceinline__ rsrc_t mkrsrc(const void* p, int bytes) {
  return __builtin_amdgcn_make_buffer_rsrc(const_cast<void*>(p), 0, bytes, 0x00020000);
}
__device__ __forceinline__ f32x4 mfma(v4u a, v4u b, f32x4 c) {
  return __builtin_amdgcn_mfma_f32_16x16x32_bf16(__builtin_bit_cast(bf16x8, a), __builtin_bit_cast(bf16x8, b), c, 0, 0, 0);
}
template <int CTRL> __device__ __forceinline__ unsigned dppu(unsigned x) {
  return (unsigned)__builtin_amdgcn_mov_dpp((int)x, CTRL, 0xf, 0xf, true);
}
template <int CTRL> __device__ __forceinline__ float dppf(float x) {
  return __builtin_bit_cast(float, __builtin_amdgcn_mov_dpp(__builtin_bit_cast(int, x), CTRL, 0xf, 0xf, true));
}
__device__ __forceinline__ unsigned cvtpk(float lo, float hi) {
  typedef __bf16 bf2 __attribute__((ext_vector_type(2)));
  bf2 r; r[0] = (__bf16)lo; r[1] = (__bf16)hi;
  return __builtin_bit_cast(unsigned, r);
}
__device__ __forceinline__ float xrow_max(float x) {
  auto s = __builtin_amdgcn_permlane16_swap(__float_as_uint(x), __float_as_uint(x), false, false);
  x = fmaxf(__uint_as_float(s[0]), __uint_as_float(s[1]));
  auto t = __builtin_amdgcn_permlane32_swap(__float_as_uint(x), __float_as_uint(x), false, false);
  return fmaxf(__uint_as_float(t[0]), __uint_as_float(t[1]));
}
__device__ __forceinline__ float xrow_sum(float x) {
  auto s = __builtin_amdgcn_permlane16_swap(__float_as_uint(x), __float_as_uint(x), false, false);
  x = __uint_as_float(s[0]) + __uint_as_float(s[1]);
  auto t = __builtin_amdgcn_permlane32_swap(__float_as_uint(x), __float_as_uint(x), false, false);
  return __uint_as_float(t[0]) + __uint_as_float(t[1]);
}
#define STAMP(k) do { } while (0)
constexpr int ROR4 = 0x124, ROR8 = 0x128, BCAST = 0x150;

constexpr int L_QP = 0;
constexpr int L_QB = L_QP + NW * 128;
constexpr int L_ACC = L_QB + NW * 64;
constexpr int L_ML = L_ACC + NW * 128;
constexpr int L_Y = L_ML + 2 * NW;
constexpr int L_END = L_Y + 2 * NW * DM;

template <int KAUX, int VAUX, int QAUX, int OAUX> __device__ __forceinline__ void attn_body(const Params& p, float* lds) {
  STAMP(0);
  const int b = blockIdx.x;
  const int hh = (b & 7) * 8 + (b >> 5), s = (b >> 3) & 3;
  const int br = hh >> 5, h = hh & 31;
  const int tid = threadIdx.x, lane = tid & 63, w = __builtin_amdgcn_readfirstlane(tid >> 6);
  const int g = lane >> 4, c = lane & 15;

  const size_t kvoff = (size_t)h * SK * (DH * 2);
  const int rot = PAT == 0 ? 0 : (int)((unsigned)(hh + 1) * 2654435761u >> 20);
  auto tile_off = [&](int k) {
    const int j = (k + rot) & 15;
    int t;
    if (PAT == 0) t = j | (w << 4) | (s << 7);
    else if (PAT == 1) t = w | (s << 3) | (j << 5);
    else if (PAT == 2) t = (j & 1) | ((w & 1) << 1) | ((s & 1) << 2) | ((w & 2) << 2) | ((j >> 1) << 4) | ((s >> 1) << 7) | ((w >> 2) << 8);
    else t = w | ((j & 1) << 3) | ((s & 1) << 4) | (((j >> 1) & 3) << 5) | ((s >> 1) << 7) | ((j >> 3) << 8);
    return t * 4096;
  };
  const rsrc_t rx = mkrsrc(p.x[br], DM * 2);
  const rsrc_t rq = mkrsrc((const char*)p.wq[br] + (size_t)w * (4 * NQL) * (NH * DH * 2) + h * (DH * 2), (4 * NQL - 1) * (NH * DH * 2) + DH * 2);
  const rsrc_t rk = mkrsrc((const char*)p.k[br] + kvoff, SK * DH * 2);
  const rsrc_t rv = mkrsrc((const char*)p.v[br] + kvoff, SK * DH * 2);
  const rsrc_t ro = mkrsrc((const char*)p.wo[br] + (size_t)(h * DH + w * (DH / NW)) * (DM * 2), (DH / NW) * DM * 2);

  const int xo = (w * (4 * NQL) + g * NQL) * 2;
  unsigned x0 = __builtin_amdgcn_raw_buffer_load_b16(rx, xo + c * 2, 0, 0);
  unsigned x1 = __builtin_amdgcn_raw_buffer_load_b16(rx, xo + 32 + (c & 3) * 2, 0, 0);

  const int q_lo = g * NQL * (NH * DH * 2) + c * 16;
  const int k_lo = (c >> 2) * 256 + g * 64 + (c & 3) * 16;
  const int v_lo = g * 1024 + c * 16;
  const int o_lo = g * (DM * 2) + c * 16;

  v4u ring[W];
  auto issue = [&](auto N) {
    constexpr int n = N;
    if constexpr (n < I_K) ring[n % W] = __builtin_amdgcn_raw_buffer_load_b128(rq, q_lo, n * (NH * DH * 2), QAUX);
    else if constexpr (n < I_V) { constexpr int i = n - I_K; ring[n % W] = __builtin_amdgcn_raw_buffer_load_b128(rk, k_lo + (i & 3) * 1024, tile_off(i >> 2), KAUX); }
    else if constexpr (n < I_O) { constexpr int i = n - I_V; ring[n % W] = __builtin_amdgcn_raw_buffer_load_b128(rv, v_lo + (i & 3) * 256, tile_off(i >> 2), VAUX); }
    else if constexpr (n < I_END) { constexpr int i = n - I_O; ring[n % W] = __builtin_amdgcn_raw_buffer_load_b128(ro, o_lo + (i % 5) * 256, (i / 5) * 4 * (DM * 2), OAUX); }
  };
  sfor<W>([&](auto N) {
    if constexpr (QBAR && N == NQL) { __builtin_amdgcn_sched_barrier(0); __builtin_amdgcn_s_barrier(); __builtin_amdgcn_sched_barrier(0); }
    issue(N); });
  __builtin_amdgcn_sched_barrier(0);
  STAMP(1);

  unsigned mask[4];
#pragma unroll
  for (int k = 0; k < 4; ++k) mask[k] = (c < 8 && (c >> 1) == k) ? ((c & 1) ? 0xffff0000u : 0x0000ffffu) : 0u;

  x0 |= x0 << 16; x1 |= x1 << 16;
  f32x4 qa = {0.f, 0.f, 0.f, 0.f};
  sfor<NQL>([&](auto N) {
    constexpr int n = N;
    const unsigned xb = n < 16 ? dppu<BCAST + (n & 15)>(x0) : dppu<BCAST + (n & 15)>(x1);
    const v4u a = {xb & mask[0], xb & mask[1], xb & mask[2], xb & mask[3]};
    qa = mfma(a, ring[n % W], qa);
    issue(std::integral_constant<int, n + W>{});
    __builtin_amdgcn_sched_barrier(0);
  });
  STAMP(2);
  if (g < 2) *reinterpret_cast<f32x4*>(&lds[L_QP + w * 128 + 8 * c + 4 * g]) = qa;
  __syncthreads();
  v4u aq[4];
  {
    f32x2 t = {0.f, 0.f};
#pragma unroll
    for (int ww = 0; ww < NW; ++ww) t += *reinterpret_cast<const f32x2*>(&lds[L_QP + ww * 128 + 2 * lane]);
    unsigned* qb = reinterpret_cast<unsigned*>(&lds[L_QB + w * 64]);
    qb[lane] = cvtpk(t[0], t[1]);
    __builtin_amdgcn_fence(__ATOMIC_RELEASE, "wavefront");
    __builtin_amdgcn_wave_barrier();
    __builtin_amdgcn_fence(__ATOMIC_ACQUIRE, "wavefront");
    const v4u qf = *reinterpret_cast<const v4u*>(qb + 4 * (4 * g + (c & 3)));
#pragma unroll
    for (int i = 0; i < 4; ++i) aq[i] = ((c >> 2) == i) ? qf : v4u{0u, 0u, 0u, 0u};
  }
  __builtin_amdgcn_sched_barrier(0);

  STAMP(3);
  float sc[NT];
  sfor<NT>([&](auto T) {
    constexpr int t = T;
    f32x4 d = {0.f, 0.f, 0.f, 0.f};
    sfor<4>([&](auto I) {
      constexpr int n = I_K + t * 4 + I;
      d = mfma(aq[I], ring[n % W], d);
      issue(std::integral_constant<int, n + W>{});
      __builtin_amdgcn_sched_barrier(0);
    });
    sc[t] = (dppf<0x00>(d[0]) + dppf<0x55>(d[1])) + (dppf<0xAA>(d[2]) + dppf<0xFF>(d[3]));
  });
  float m = sc[0];
#pragma unroll
  for (int t = 1; t < NT; ++t) m = fmaxf(m, sc[t]);
  m = fmaxf(m, dppf<ROR4>(m)); m = fmaxf(m, dppf<ROR8>(m)); m = xrow_max(m);
  const float mc = m * C2;
  float l = 0.f;
  unsigned pp[NT];
#pragma unroll
  for (int t = 0; t < NT; ++t) { const float e = __builtin_amdgcn_exp2f(fmaf(sc[t], C2, -mc)); l += e; pp[t] = cvtpk(e, e); }
  l += dppf<ROR4>(l); l += dppf<ROR8>(l); l = xrow_sum(l);
  __builtin_amdgcn_sched_barrier(0);

  STAMP(4);
  f32x4 acc = {0.f, 0.f, 0.f, 0.f};
  sfor<NKL>([&](auto I) {
    constexpr int i = I, n = I_V + i;
    const unsigned pb = dppu<BCAST + 4 * (i & 3)>(pp[i >> 2]);
    const v4u a = {pb & mask[0], pb & mask[1], pb & mask[2], pb & mask[3]};
    acc = mfma(a, ring[n % W], acc);
    issue(std::integral_constant<int, n + W>{});
    __builtin_amdgcn_sched_barrier(0);
  });
  STAMP(5);
  if (g < 2) *reinterpret_cast<f32x4*>(&lds[L_ACC + w * 128 + 8 * c + 4 * g]) = acc;
  if (lane == 0) *reinterpret_cast<f32x2*>(&lds[L_ML + 2 * w]) = f32x2{mc, l};
  __syncthreads();

  STAMP(6);
  float M = -INFINITY, L = 0.f, o = 0.f;
  {
    f32x2 ml[NW];
#pragma unroll
    for (int ww = 0; ww < NW; ++ww) { ml[ww] = *reinterpret_cast<const f32x2*>(&lds[L_ML + 2 * ww]); M = fmaxf(M, ml[ww][0]); }
    const int row = w * (DH / NW) + 4 * (c & 3) + g;
#pragma unroll
    for (int ww = 0; ww < NW; ++ww) { const float wt = __builtin_amdgcn_exp2f(ml[ww][0] - M); L = fmaf(wt, ml[ww][1], L); o = fmaf(wt, lds[L_ACC + ww * 128 + row], o); }
  }
  const unsigned ohi = cvtpk(o, o);
  const float ores = o - __uint_as_float(ohi & 0xffff0000u);
  const unsigned olo = cvtpk(ores, ores);
  unsigned mask2[4];
#pragma unroll
  for (int k = 0; k < 4; ++k) mask2[k] = (((c & 7) >> 1) == k) ? ((c & 1) ? 0xffff0000u : 0x0000ffffu) : 0u;
  f32x4 y[5];
#pragma unroll
  for (int cg = 0; cg < 5; ++cg) y[cg] = f32x4{0.f, 0.f, 0.f, 0.f};
  sfor<DH / NW / 4>([&](auto RG) {
    constexpr int rg = RG;
    const unsigned hb = dppu<BCAST + rg>(ohi), lb = dppu<BCAST + rg>(olo);
    const unsigned ob = c < 8 ? hb : lb;
    const v4u a = {ob & mask2[0], ob & mask2[1], ob & mask2[2], ob & mask2[3]};
    sfor<5>([&](auto CG) { constexpr int n = I_O + rg * 5 + CG; y[CG] = mfma(a, ring[n % W], y[CG]); });
  });
#pragma unroll
  for (int cg = 0; cg < 5; ++cg) *reinterpret_cast<f32x4*>(&lds[L_Y + (2 * w + (g >> 1)) * DM + cg * 128 + 8 * c + 4 * (g & 1)]) = y[cg];
  __syncthreads();
  float* rec = p.ws + (size_t)(hh * NSPLIT + s) * PSTRIDE;
  if (tid < DM / 2) {
    f32x2 t = {0.f, 0.f};
#pragma unroll
    for (int j = 0; j < 2 * NW; ++j) t += *reinterpret_cast<const f32x2*>(&lds[L_Y + j * DM + 2 * tid]);
    *reinterpret_cast<f32x2*>(rec + 2 * tid) = t;
  } else if (tid == DM / 2) {
    *reinterpret_cast<f32x2*>(rec + DM) = f32x2{M, L};
  }
}

__global__ void __launch_bounds__(NTHR, 2) attn_main(Params p) {
  __shared__ __attribute__((aligned(16))) float lds[L_END];
  if ((blockIdx.x & 7) < 4) attn_body<AK, AV, AQ, AO>(p, lds); else attn_body<BK, BV, BQ, BO>(p, lds);
}

__global__ void __launch_bounds__(512) attn_reduce(const float* __restrict__ ws, unsigned short* __restrict__ out) {
  __shared__ __attribute__((aligned(16))) float red[8][16];
  const int br = blockIdx.y, c0 = blockIdx.x * 16;
  const int tid = threadIdx.x, lane = tid & 63, w = tid >> 6;
  const int part = tid >> 2, q = tid & 3;
  const float* base = ws + (size_t)br * (NH * NSPLIT) * PSTRIDE;
  const f32x4 yv = *reinterpret_cast<const f32x4*>(base + (size_t)part * PSTRIDE + c0 + 4 * q);
  const f32x2 ml = *reinterpret_cast<const f32x2*>(base + (size_t)part * PSTRIDE + DM);
  float M = ml[0]; M = fmaxf(M, dppf<ROR4>(M)); M = fmaxf(M, dppf<ROR8>(M));
  const float wt = __builtin_amdgcn_exp2f(ml[0] - M);
  float L = wt * ml[1]; L += dppf<ROR4>(L); L += dppf<ROR8>(L);
  const float f = wt / L;
  f32x4 r;
#pragma unroll
  for (int j = 0; j < 4; ++j) { float t = yv[j] * f; t += dppf<ROR4>(t); t += dppf<ROR8>(t); r[j] = xrow_sum(t); }
  if (lane < 4) *reinterpret_cast<f32x4*>(&red[w][4 * lane]) = r;
  __syncthreads();
  if (tid < 8) {
    float a = 0.f, bb = 0.f;
#pragma unroll
    for (int j = 0; j < 8; ++j) { a += red[j][2 * tid]; bb += red[j][2 * tid + 1]; }
    *reinterpret_cast<unsigned*>(out + br * DM + c0 + 2 * tid) = cvtpk(a, bb);
  }
}

extern "C" void kernel_launch(void* const* d_in, const int* in_sizes, int n_in,
                              void* d_out, int out_size, void* d_ws, size_t ws_size,
                              hipStream_t stream) {
  Params p;
  for (int i = 0; i < 2; ++i) {
    p.x[i] = d_in[5 * i + 0]; p.k[i] = d_in[5 * i + 1]; p.v[i] = d_in[5 * i + 2]; p.wq[i] = d_in[5 * i + 3]; p.wo[i] = d_in[5 * i + 4];
  }
  p.ws = (float*)d_ws; p.out = (unsigned short*)d_out;
  hipLaunchKernelGGL(attn_main, dim3(256), dim3(NTHR), 0, stream, p);
  hipLaunchKernelGGL(attn_reduce, dim3(DM / 16, 2), dim3(512), 0, stream, (const float*)d_ws, (unsigned short*)d_out);
}
```

```cpp
#define PAT 3
#define QBAR 1
#define RING 32
#define WQ_AUX 2
#define WO_AUX 2
#define SPIN_TICKS 0
#include <hip/hip_runtime.h>
#include <hip/hip_bf16.h>
#include <utility>

#define KV_AUX 2

typedef unsigned v4u __attribute__((ext_vector_type(4)));
typedef float f32x4 __attribute__((ext_vector_type(4)));
typedef float f32x2 __attribute__((ext_vector_type(2)));
typedef short bf16x8 __attribute__((ext_vector_type(8)));
typedef __amdgpu_buffer_rsrc_t rsrc_t;

constexpr int DM = 640, NH = 32, SK = 8192, DH = 128;
constexpr int NSPLIT = 4, NW = 8, NTHR = NW * 64;
constexpr int KEYS_WG = SK / NSPLIT, KEYS_W = KEYS_WG / NW;
constexpr int NT = KEYS_W / 16;
constexpr int NQL = DM / NW / 4;
constexpr int NKL = NT * 4;
constexpr int NOL = (DH / NW / 4) * 5;
constexpr int I_K = NQL, I_V = I_K + NKL, I_O = I_V + NKL, I_END = I_O + NOL;
constexpr int W = RING;
constexpr float C2 = 0.088388347648318440f * 1.4426950408889634f;
constexpr int PSTRIDE = 656;

struct Params {
  const void* x[2]; const void* k[2]; const void* v[2]; const void* wq[2]; const void* wo[2];
  float* ws; unsigned short* out;
};

template <int N, typename F, int... Is>
__device__ __forceinline__ void sfor_impl(F&& f, std::integer_sequence<int, Is...>) { (f(std::integral_constant<int, Is>{}), ...); }
template <int N, typename F> __device__ __forceinline__ void sfor(F&& f) { sfor_impl<N>(f, std::make_integer_sequence<int, N>{}); }

__device__ __forceinline__ rsrc_t mkrsrc(const void* p, int bytes) {
  return __builtin_amdgcn_make_buffer_rsrc(const_cast<void*>(p), 0, bytes, 0x00020000);
}
__device__ __forceinline__ f32x4 mfma(v4u a, v4u b, f32x4 c) {
  return __builtin_amdgcn_mfma_f32_16x16x32_bf16(__builtin_bit_cast(bf16x8, a), __builtin_bit_cast(bf16x8, b), c, 0, 0, 0);
}
template <int CTRL> __device__ __forceinline__ unsigned dppu(unsigned x) {
  return (unsigned)__builtin_amdgcn_mov_dpp((int)x, CTRL, 0xf, 0xf, true);
}
template <int CTRL> __device__ __forceinline__ float dppf(float x) {
  return __builtin_bit_cast(float, __builtin_amdgcn_mov_dpp(__builtin_bit_cast(int, x), CTRL, 0xf, 0xf, true));
}
__device__ __forceinline__ unsigned cvtpk(float lo, float hi) {
  typedef __bf16 bf2 __attribute__((ext_vector_type(2)));
  bf2 r; r[0] = (__bf16)lo; r[1] = (__bf16)hi;
  return __builtin_bit_cast(unsigned, r);
}
__device__ __forceinline__ float xrow_max(float x) {
  auto s = __builtin_amdgcn_permlane16_swap(__float_as_uint(x), __float_as_uint(x), false, false);
  x = fmaxf(__uint_as_float(s[0]), __uint_as_float(s[1]));
  auto t = __builtin_amdgcn_permlane32_swap(__float_as_uint(x), __float_as_uint(x), false, false);
  return fmaxf(__uint_as_float(t[0]), __uint_as_float(t[1]));
}
__device__ __forceinline__ float xrow_sum(float x) {
  auto s = __builtin_amdgcn_permlane16_swap(__float_as_uint(x), __float_as_uint(x), false, false);
  x = __uint_as_float(s[0]) + __uint_as_float(s[1]);
  auto t = __builtin_amdgcn_permlane32_swap(__float_as_uint(x), __float_as_uint(x), false, false);
  return __uint_as_float(t[0]) + __uint_as_float(t[1]);
}
#define STAMP(k) do { } while (0)
constexpr int ROR4 = 0x124, ROR8 = 0x128, BCAST = 0x150;

constexpr int L_QP = 0;
constexpr int L_QB = L_QP + NW * 128;
constexpr int L_ACC = L_QB + NW * 64;
constexpr int L_ML = L_ACC + NW * 128;
constexpr int L_Y = L_ML + 2 * NW;
constexpr int L_END = L_Y + 2 * NW * DM;

__global__ void __launch_bounds__(NTHR, 2) attn_main(Params p) {
  __shared__ __attribute__((aligned(16))) float lds[L_END];
  const unsigned long long tstart = __builtin_amdgcn_s_memrealtime();
  const int b = blockIdx.x;
  const int hh = (b & 7) * 8 + (b >> 5), s = (b >> 3) & 3;
  const int br = hh >> 5, h = hh & 31;
  const int tid = threadIdx.x, lane = tid & 63, w = __builtin_amdgcn_readfirstlane(tid >> 6);
  const int g = lane >> 4, c = lane & 15;

  const size_t kvoff = (size_t)h * SK * (DH * 2);
  const int rot = PAT == 0 ? 0 : (int)((unsigned)(hh + 1) * 2654435761u >> 20);
  auto tile_off = [&](int k) {
    const int j = (k + rot) & 15;
    int t;
    if (PAT == 0) t = j | (w << 4) | (s << 7);
    else if (PAT == 1) t = w | (s << 3) | (j << 5);
    else if (PAT == 2) t = (j & 1) | ((w & 1) << 1) | ((s & 1) << 2) | ((w & 2) << 2) | ((j >> 1) << 4) | ((s >> 1) << 7) | ((w >> 2) << 8);
    else t = w | ((j & 1) << 3) | ((s & 1) << 4) | (((j >> 1) & 3) << 5) | ((s >> 1) << 7) | ((j >> 3) << 8);
    return t * 4096;
  };
  const rsrc_t rx = mkrsrc(p.x[br], DM * 2);
  const rsrc_t rq = mkrsrc((const char*)p.wq[br] + (size_t)w * (4 * NQL) * (NH * DH * 2) + h * (DH * 2), (4 * NQL - 1) * (NH * DH * 2) + DH * 2);
  const rsrc_t rk = mkrsrc((const char*)p.k[br] + kvoff, SK * DH * 2);
  const rsrc_t rv = mkrsrc((const char*)p.v[br] + kvoff, SK * DH * 2);
  const rsrc_t ro = mkrsrc((const char*)p.wo[br] + (size_t)(h * DH + w * (DH / NW)) * (DM * 2), (DH / NW) * DM * 2);

  const int xo = (w * (4 * NQL) + g * NQL) * 2;
  unsigned x0 = __builtin_amdgcn_raw_buffer_load_b16(rx, xo + c * 2, 0, 0);
  unsigned x1 = __builtin_amdgcn_raw_buffer_load_b16(rx, xo + 32 + (c & 3) * 2, 0, 0);

  const int q_lo = g * NQL * (NH * DH * 2) + c * 16;
  const int k_lo = (c >> 2) * 256 + g * 64 + (c & 3) * 16;
  const int v_lo = g * 1024 + c * 16;
  const int o_lo = g * (DM * 2) + c * 16;

  v4u ring[W];
  auto issue = [&](auto N) {
    constexpr int n = N;
    if constexpr (n < I_K) ring[n % W] = __builtin_amdgcn_raw_buffer_load_b128(rq, q_lo, n * (NH * DH * 2), WQ_AUX);
    else if constexpr (n < I_V) { constexpr int i = n - I_K; ring[n % W] = __builtin_amdgcn_raw_buffer_load_b128(rk, k_lo + (i & 3) * 1024, tile_off(i >> 2), KV_AUX); }
    else if constexpr (n < I_O) { constexpr int i = n - I_V; ring[n % W] = __builtin_amdgcn_raw_buffer_load_b128(rv, v_lo + (i & 3) * 256, tile_off(i >> 2), KV_AUX); }
    else if constexpr (n < I_END) { constexpr int i = n - I_O; ring[n % W] = __builtin_amdgcn_raw_buffer_load_b128(ro, o_lo + (i % 5) * 256, (i / 5) * 4 * (DM * 2), WO_AUX); }
  };
  sfor<W>([&](auto N) {
    if constexpr (QBAR && N == NQL) { __builtin_amdgcn_sched_barrier(0); __builtin_amdgcn_s_barrier(); __builtin_amdgcn_sched_barrier(0); }
    issue(N); });
  __builtin_amdgcn_sched_barrier(0);
  STAMP(1);

  unsigned mask[4];
#pragma unroll
  for (int k = 0; k < 4; ++k) mask[k] = (c < 8 && (c >> 1) == k) ? ((c & 1) ? 0xffff0000u : 0x0000ffffu) : 0u;

  x0 |= x0 << 16; x1 |= x1 << 16;
  f32x4 qa = {0.f, 0.f, 0.f, 0.f};
  sfor<NQL>([&](auto N) {
    constexpr int n = N;
    const unsigned xb = n < 16 ? dppu<BCAST + (n & 15)>(x0) : dppu<BCAST + (n & 15)>(x1);
    const v4u a = {xb & mask[0], xb & mask[1], xb & mask[2], xb & mask[3]};
    qa = mfma(a, ring[n % W], qa);
    issue(std::integral_constant<int, n + W>{});
    __builtin_amdgcn_sched_barrier(0);
  });
  STAMP(2);
  if (g < 2) *reinterpret_cast<f32x4*>(&lds[L_QP + w * 128 + 8 * c + 4 * g]) = qa;
  __syncthreads();
  v4u aq[4];
  {
    f32x2 t = {0.f, 0.f};
#pragma unroll
    for (int ww = 0; ww < NW; ++ww) t += *reinterpret_cast<const f32x2*>(&lds[L_QP + ww * 128 + 2 * lane]);
    unsigned* qb = reinterpret_cast<unsigned*>(&lds[L_QB + w * 64]);
    qb[lane] = cvtpk(t[0], t[1]);
    __builtin_amdgcn_fence(__ATOMIC_RELEASE, "wavefront");
    __builtin_amdgcn_wave_barrier();
    __builtin_amdgcn_fence(__ATOMIC_ACQUIRE, "wavefront");
    const v4u qf = *reinterpret_cast<const v4u*>(qb + 4 * (4 * g + (c & 3)));
#pragma unroll
    for (int i = 0; i < 4; ++i) aq[i] = ((c >> 2) == i) ? qf : v4u{0u, 0u, 0u, 0u};
  }
  __builtin_amdgcn_sched_barrier(0);

  STAMP(3);
  float sc[NT];
  sfor<NT>([&](auto T) {
    constexpr int t = T;
    f32x4 d = {0.f, 0.f, 0.f, 0.f};
    sfor<4>([&](auto I) {
      constexpr int n = I_K + t * 4 + I;
      d = mfma(aq[I], ring[n % W], d);
      issue(std::integral_constant<int, n + W>{});
      __builtin_amdgcn_sched_barrier(0);
    });
    sc[t] = (dppf<0x00>(d[0]) + dppf<0x55>(d[1])) + (dppf<0xAA>(d[2]) + dppf<0xFF>(d[3]));
  });
  float m = sc[0];
#pragma unroll
  for (int t = 1; t < NT; ++t) m = fmaxf(m, sc[t]);
  m = fmaxf(m, dppf<ROR4>(m)); m = fmaxf(m, dppf<ROR8>(m)); m = xrow_max(m);
  const float mc = m * C2;
  float l = 0.f;
  unsigned pp[NT];
#pragma unroll
  for (int t = 0; t < NT; ++t) { const float e = __builtin_amdgcn_exp2f(fmaf(sc[t], C2, -mc)); l += e; pp[t] = cvtpk(e, e); }
  l += dppf<ROR4>(l); l += dppf<ROR8>(l); l = xrow_sum(l);
  __builtin_amdgcn_sched_barrier(0);

  STAMP(4);
  f32x4 acc = {0.f, 0.f, 0.f, 0.f};
  sfor<NKL>([&](auto I) {
    constexpr int i = I, n = I_V + i;
    const unsigned pb = dppu<BCAST + 4 * (i & 3)>(pp[i >> 2]);
    const v4u a = {pb & mask[0], pb & mask[1], pb & mask[2], pb & mask[3]};
    acc = mfma(a, ring[n % W], acc);
    issue(std::integral_constant<int, n + W>{});
    __builtin_amdgcn_sched_barrier(0);
  });
  STAMP(5);
  if (g < 2) *reinterpret_cast<f32x4*>(&lds[L_ACC + w * 128 + 8 * c + 4 * g]) = acc;
  if (lane == 0) *reinterpret_cast<f32x2*>(&lds[L_ML + 2 * w]) = f32x2{mc, l};
  __syncthreads();

  STAMP(6);
  float M = -INFINITY, L = 0.f, o = 0.f;
  {
    f32x2 ml[NW];
#pragma unroll
    for (int ww = 0; ww < NW; ++ww) { ml[ww] = *reinterpret_cast<const f32x2*>(&lds[L_ML + 2 * ww]); M = fmaxf(M, ml[ww][0]); }
    const int row = w * (DH / NW) + 4 * (c & 3) + g;
#pragma unroll
    for (int ww = 0; ww < NW; ++ww) { const float wt = __builtin_amdgcn_exp2f(ml[ww][0] - M); L = fmaf(wt, ml[ww][1], L); o = fmaf(wt, lds[L_ACC + ww * 128 + row], o); }
  }
  const unsigned ohi = cvtpk(o, o);
  const float ores = o - __uint_as_float(ohi & 0xffff0000u);
  const unsigned olo = cvtpk(ores, ores);
  unsigned mask2[4];
#pragma unroll
  for (int k = 0; k < 4; ++k) mask2[k] = (((c & 7) >> 1) == k) ? ((c & 1) ? 0xffff0000u : 0x0000ffffu) : 0u;
  f32x4 y[5];
#pragma unroll
  for (int cg = 0; cg < 5; ++cg) y[cg] = f32x4{0.f, 0.f, 0.f, 0.f};
  sfor<DH / NW / 4>([&](auto RG) {
    constexpr int rg = RG;
    const unsigned hb = dppu<BCAST + rg>(ohi), lb = dppu<BCAST + rg>(olo);
    const unsigned ob = c < 8 ? hb : lb;
    const v4u a = {ob & mask2[0], ob & mask2[1], ob & mask2[2], ob & mask2[3]};
    sfor<5>([&](auto CG) { constexpr int n = I_O + rg * 5 + CG; y[CG] = mfma(a, ring[n % W], y[CG]); });
  });
#pragma unroll
  for (int cg = 0; cg < 5; ++cg) *reinterpret_cast<f32x4*>(&lds[L_Y + (2 * w + (g >> 1)) * DM + cg * 128 + 8 * c + 4 * (g & 1)]) = y[cg];
  __syncthreads();
  float* rec = p.ws + (size_t)(hh * NSPLIT + s) * PSTRIDE;
  if (tid < DM / 2) {
    f32x2 t = {0.f, 0.f};
#pragma unroll
    for (int j = 0; j < 2 * NW; ++j) t += *reinterpret_cast<const f32x2*>(&lds[L_Y + j * DM + 2 * tid]);
    *reinterpret_cast<f32x2*>(rec + 2 * tid) = t;
  } else if (tid == DM / 2) {
    *reinterpret_cast<f32x2*>(rec + DM) = f32x2{M, L};
  }
  while (__builtin_amdgcn_s_memrealtime() - tstart < SPIN_TICKS) __builtin_amdgcn_s_sleep(1);
}

__global__ void __launch_bounds__(512) attn_reduce(const float* __restrict__ ws, unsigned short* __restrict__ out) {
  __shared__ __attribute__((aligned(16))) float red[8][16];
  const int br = blockIdx.y, c0 = blockIdx.x * 16;
  const int tid = threadIdx.x, lane = tid & 63, w = tid >> 6;
  const int part = tid >> 2, q = tid & 3;
  const float* base = ws + (size_t)br * (NH * NSPLIT) * PSTRIDE;
  const f32x4 yv = *reinterpret_cast<const f32x4*>(base + (size_t)part * PSTRIDE + c0 + 4 * q);
  const f32x2 ml = *reinterpret_cast<const f32x2*>(base + (size_t)part * PSTRIDE + DM);
  float M = ml[0]; M = fmaxf(M, dppf<ROR4>(M)); M = fmaxf(M, dppf<ROR8>(M));
  const float wt = __builtin_amdgcn_exp2f(ml[0] - M);
  float L = wt * ml[1]; L += dppf<ROR4>(L); L += dppf<ROR8>(L);
  const float f = wt / L;
  f32x4 r;
#pragma unroll
  for (int j = 0; j < 4; ++j) { float t = yv[j] * f; t += dppf<ROR4>(t); t += dppf<ROR8>(t); r[j] = xrow_sum(t); }
  if (lane < 4) *reinterpret_cast<f32x4*>(&red[w][4 * lane]) = r;
  __syncthreads();
  if (tid < 8) {
    float a = 0.f, bb = 0.f;
#pragma unroll
    for (int j = 0; j < 8; ++j) { a += red[j][2 * tid]; bb += red[j][2 * tid + 1]; }
    *reinterpret_cast<unsigned*>(out + br * DM + c0 + 2 * tid) = cvtpk(a, bb);
  }
}

extern "C" void kernel_launch(void* const* d_in, const int* in_sizes, int n_in,
                              void* d_out, int out_size, void* d_ws, size_t ws_size,
                              hipStream_t stream) {
  Params p;
  for (int i = 0; i < 2; ++i) {
    p.x[i] = d_in[5 * i + 0]; p.k[i] = d_in[5 * i + 1]; p.v[i] = d_in[5 * i + 2]; p.wq[i] = d_in[5 * i + 3]; p.wo[i] = d_in[5 * i + 4];
  }
  p.ws = (float*)d_ws; p.out = (unsigned short*)d_out;
  hipLaunchKernelGGL(attn_main, dim3(256), dim3(NTHR), 0, stream, p);
  hipLaunchKernelGGL(attn_reduce, dim3(DM / 16, 2), dim3(512), 0, stream, (const float*)d_ws, (unsigned short*)d_out);
}
```

```cpp
#include <hip/hip_runtime.h>
#include <hip/hip_bf16.h>
#include <utility>

#define RING 20
#define KV_AUX 2
#define W_AUX 0
#define PAT 3
#define POLL_SLEEP 4
#define MIMIC 1
#define QROT 1
#define OROT 1
#define WO_AUX W_AUX
#define HSHIFT 0
#define PRIO 0
#define KA 4
#define KB 4
#define KC 4
#define SPLITP 0

typedef unsigned v4u __attribute__((ext_vector_type(4)));
typedef float f32x4 __attribute__((ext_vector_type(4)));
typedef float f32x2 __attribute__((ext_vector_type(2)));
typedef short bf16x8 __attribute__((ext_vector_type(8)));
typedef __amdgpu_buffer_rsrc_t rsrc_t;

constexpr int DM = 640, NH = 32, SK = 8192, DH = 128;
constexpr int NSPLIT = 4, NW = 8, NTHR = NW * 64;
constexpr int NT = SK / NSPLIT / NW / 16;
constexpr int NQL = DM / NW / 4;
constexpr int NKL = NT * 4;
constexpr int NOL = (DH / NW / 4) * 5;
constexpr int W = RING;
constexpr float SCALE = 0.088388347648318440f, LOG2E = 1.4426950408889634f;
constexpr int AUX_SC1 = 16;
constexpr int WS_EPOCH = 0, WS_ML = 4096, WS_Y = 8192, WS_BYTES = WS_Y + 2 * 128 * 320 * 16;
constexpr int NRED = DM / 8;

struct Params {
  const void* x[2]; const void* k[2]; const void* v[2]; const void* wq[2]; const void* wo[2];
  void* ws; unsigned short* out;
};

template <int N, typename F, int... Is>
__device__ __forceinline__ void sfor_impl(F&& f, std::integer_sequence<int, Is...>) { (f(std::integral_constant<int, Is>{}), ...); }
template <int N, typename F> __device__ __forceinline__ void sfor(F&& f) { sfor_impl<N>(f, std::make_integer_sequence<int, N>{}); }

__device__ __forceinline__ rsrc_t mkrsrc(const void* p, int bytes) {
  return __builtin_amdgcn_make_buffer_rsrc(const_cast<void*>(p), 0, bytes, 0x00020000);
}
__device__ __forceinline__ f32x4 mfma(v4u a, v4u b, f32x4 c) {
  return __builtin_amdgcn_mfma_f32_16x16x32_bf16(__builtin_bit_cast(bf16x8, a), __builtin_bit_cast(bf16x8, b), c, 0, 0, 0);
}
template <int CTRL> __device__ __forceinline__ unsigned dppu(unsigned x) {
  return (unsigned)__builtin_amdgcn_mov_dpp((int)x, CTRL, 0xf, 0xf, true);
}
template <int CTRL> __device__ __forceinline__ float dppf(float x) {
  return __builtin_bit_cast(float, __builtin_amdgcn_mov_dpp(__builtin_bit_cast(int, x), CTRL, 0xf, 0xf, true));
}
__device__ __forceinline__ unsigned cvtpk(float lo, float hi) {
  typedef __bf16 bf2 __attribute__((ext_vector_type(2)));
  bf2 r; r[0] = (__bf16)lo; r[1] = (__bf16)hi;
  return __builtin_bit_cast(unsigned, r);
}
__device__ __forceinline__ float rbf(float x) { return __uint_as_float(cvtpk(0.f, x)); }
__device__ __forceinline__ float xrow_max(float x) {
  auto s = __builtin_amdgcn_permlane16_swap(__float_as_uint(x), __float_as_uint(x), false, false);
  x = fmaxf(__uint_as_float(s[0]), __uint_as_float(s[1]));
  auto t = __builtin_amdgcn_permlane32_swap(__float_as_uint(x), __float_as_uint(x), false, false);
  return fmaxf(__uint_as_float(t[0]), __uint_as_float(t[1]));
}
__device__ __forceinline__ float xrow_sum(float x) {
  auto s = __builtin_amdgcn_permlane16_swap(__float_as_uint(x), __float_as_uint(x), false, false);
  x = __uint_as_float(s[0]) + __uint_as_float(s[1]);
  auto t = __builtin_amdgcn_permlane32_swap(__float_as_uint(x), __float_as_uint(x), false, false);
  return __uint_as_float(t[0]) + __uint_as_float(t[1]);
}
#define SB() __builtin_amdgcn_sched_barrier(0)
constexpr int ROR4 = 0x124, ROR8 = 0x128, BCAST = 0x150;

constexpr int L_QP = 0;
constexpr int L_QB = L_QP + NW * 128;
constexpr int L_ACC = L_QB + NW * 64;
constexpr int L_ML = L_ACC + NW * 128;
constexpr int L_WT = L_ML + 2 * NW;
constexpr int L_CNT = L_WT + NW;
constexpr int L_RED = L_CNT + 4;
constexpr int L_Y = L_RED + NW * 8;
constexpr int L_END = L_Y + 2 * NW * DM;

__global__ void __launch_bounds__(NTHR, 2) attn_fused(Params p) {
  __shared__ __attribute__((aligned(16))) float lds[L_END];
  const int b = blockIdx.x;
  const int hh = (b & 7) * 8 + (((b >> 5) + HSHIFT) & 7), s = (b >> 3) & 3;
  const int br = hh >> 5, h = hh & 31;
  const int tid = threadIdx.x, lane = tid & 63, w = __builtin_amdgcn_readfirstlane(tid >> 6);
  const int g = lane >> 4, c = lane & 15;

  const size_t kvoff = (size_t)h * SK * (DH * 2);
  const int rot = (int)((unsigned)(hh + 1) * 2654435761u >> 20);
  auto tile_off = [&](int k) {
    const int j = (k + rot) & 15;
    int t;
    if (PAT == 1) t = w | (s << 3) | (j << 5);
    else if (PAT == 2) t = (j & 1) | ((w & 1) << 1) | ((s & 1) << 2) | ((w & 2) << 2) | ((j >> 1) << 4) | ((s >> 1) << 7) | ((w >> 2) << 8);
    else t = w | ((j & 1) << 3) | ((s & 1) << 4) | (((j >> 1) & 3) << 5) | ((s >> 1) << 7) | ((j >> 3) << 8);
    return t * 4096;
  };
  const rsrc_t rx = mkrsrc(p.x[br], DM * 2);
  const rsrc_t rq = mkrsrc((const char*)p.wq[br] + (size_t)w * (4 * NQL) * (NH * DH * 2) + h * (DH * 2), (4 * NQL - 1) * (NH * DH * 2) + DH * 2);
  const rsrc_t rk = mkrsrc((const char*)p.k[br] + kvoff, SK * DH * 2);
  const rsrc_t rv = mkrsrc((const char*)p.v[br] + kvoff, SK * DH * 2);
  const rsrc_t ro = mkrsrc((const char*)p.wo[br] + (size_t)(h * DH + w * (DH / NW)) * (DM * 2), (DH / NW) * DM * 2);
  const rsrc_t rw = mkrsrc(p.ws, WS_BYTES);
  unsigned short* outp = p.out + br * DM;
  asm volatile("" : "+s"(outp));

  const int q_lo = g * NQL * (NH * DH * 2) + c * 16;
  const int k_lo = (c >> 2) * 256 + g * 64 + (c & 3) * 16;
  const int v_lo = g * 1024 + c * 16;
  const int o_lo = g * (DM * 2) + c * 16;
  const int sq = QROT ? s : 0, so = OROT ? s : 0;
  v4u ring[W], aux[NQL];
  auto issue_q = [&](auto N) { constexpr int n = N;
    aux[n] = __builtin_amdgcn_raw_buffer_load_b128(rq, q_lo, ((((n / 5) + sq) & 3) * 5 + n % 5) * (NH * DH * 2), W_AUX); };
  auto issue_o = [&](auto N) { constexpr int n = N;
    aux[n] = __builtin_amdgcn_raw_buffer_load_b128(ro, o_lo + (n % 5) * 256, (((n / 5) + so) & 3) * 4 * (DM * 2), WO_AUX); };
  auto issue = [&](auto N) {
    constexpr int n = N;
    if constexpr (n < NKL) ring[n % W] = __builtin_amdgcn_raw_buffer_load_b128(rk, k_lo + (n & 3) * 1024, tile_off(n >> 2), KV_AUX);
    else if constexpr (n < 2 * NKL) { constexpr int i = n - NKL; ring[n % W] = __builtin_amdgcn_raw_buffer_load_b128(rv, v_lo + (i & 3) * 256, tile_off(i >> 2), KV_AUX); }
  };
  const int jq0 = ((c / 5 + sq) & 3) * 5 + c % 5, jq1 = ((3 + sq) & 3) * 5 + 1 + (c & 3);
  const int xo = (w * (4 * NQL) + g * NQL) * 2;
  unsigned x0 = __builtin_amdgcn_raw_buffer_load_b16(rx, xo + jq0 * 2, 0, 0);
  unsigned x1 = __builtin_amdgcn_raw_buffer_load_b16(rx, xo + jq1 * 2, 0, 0);
  const unsigned epoch = __builtin_amdgcn_raw_buffer_load_b32(rw, WS_EPOCH + br * 256, 0, AUX_SC1);
  SB();
  constexpr int KR[5] = {0, KA, KA + KB, KA + KB + KC, W};
  sfor<4>([&](auto Q) {
    if constexpr (PRIO && Q < 3) __builtin_amdgcn_s_setprio(3 - Q);
    sfor<5>([&](auto J) { issue_q(std::integral_constant<int, Q * 5 + J>{}); });
    SB();
    if constexpr (Q < 3) sfor<KR[Q + 1] - KR[Q]>([&](auto J) { issue(std::integral_constant<int, KR[Q] + J>{}); });
    SB();
  });
  if constexpr (PRIO) __builtin_amdgcn_s_setprio(0);

  unsigned mask[4];
#pragma unroll
  for (int k = 0; k < 4; ++k) mask[k] = (c < 8 && (c >> 1) == k) ? ((c & 1) ? 0xffff0000u : 0x0000ffffu) : 0u;

  unsigned tag;
  auto trailing = [&]() { SB(); sfor<W - KR[3]>([&](auto N) { issue(std::integral_constant<int, KR[3] + N>{}); }); SB(); };
  auto qproj = [&]() {
    x0 |= x0 << 16; x1 |= x1 << 16;
    tag = __builtin_amdgcn_readfirstlane(epoch) + 1u;
    f32x4 qa = {0.f, 0.f, 0.f, 0.f};
    sfor<NQL>([&](auto N) {
      constexpr int n = N;
      const unsigned xb = n < 16 ? dppu<BCAST + (n & 15)>(x0) : dppu<BCAST + (n & 15)>(x1);
      const v4u a = {xb & mask[0], xb & mask[1], xb & mask[2], xb & mask[3]};
      qa = mfma(a, aux[n], qa);
    });
    *reinterpret_cast<f32x4*>(&lds[(g < 2 ? L_QP : L_Y - 8) + w * 128 + 8 * c + 4 * g]) = qa;
  };
  if (SPLITP == 0 || (SPLITP == 1 && w < 4)) { trailing(); qproj(); } else { qproj(); trailing(); }
  if (tid == 0) *reinterpret_cast<unsigned*>(&lds[L_CNT]) = 0u;
  __syncthreads();
  v4u aq[4];
  {
    f32x2 t = {0.f, 0.f};
#pragma unroll
    for (int ww = 0; ww < NW; ++ww) t += *reinterpret_cast<const f32x2*>(&lds[L_QP + ww * 128 + 2 * lane]);
    unsigned* qb = reinterpret_cast<unsigned*>(&lds[L_QB + w * 64]);
    qb[lane] = cvtpk(t[0], t[1]);
    __builtin_amdgcn_fence(__ATOMIC_RELEASE, "wavefront");
    __builtin_amdgcn_wave_barrier();
    __builtin_amdgcn_fence(__ATOMIC_ACQUIRE, "wavefront");
    const v4u qf = *reinterpret_cast<const v4u*>(qb + 4 * (4 * g + (c & 3)));
#pragma unroll
    for (int i = 0; i < 4; ++i) aq[i] = ((c >> 2) == i) ? qf : v4u{0u, 0u, 0u, 0u};
  }
  SB();

  float sc[NT];
  sfor<NT>([&](auto T) {
    constexpr int t = T;
    f32x4 d = {0.f, 0.f, 0.f, 0.f};
    sfor<4>([&](auto I) {
      constexpr int n = t * 4 + I;
      d = mfma(aq[I], ring[n % W], d);
      issue(std::integral_constant<int, n + W>{});
      if constexpr (n % 2 == 0 && n / 2 < NOL) issue_o(std::integral_constant<int, n / 2>{});
      SB();
    });
    float v = (dppf<0x00>(d[0]) + dppf<0x55>(d[1])) + (dppf<0xAA>(d[2]) + dppf<0xFF>(d[3]));
    sc[t] = MIMIC ? rbf(rbf(v) * SCALE) : v * SCALE;
  });
  float m = sc[0];
#pragma unroll
  for (int t = 1; t < NT; ++t) m = fmaxf(m, sc[t]);
  m = fmaxf(m, dppf<ROR4>(m)); m = fmaxf(m, dppf<ROR8>(m)); m = xrow_max(m);
  float l = 0.f;
  unsigned pp[NT];
#pragma unroll
  for (int t = 0; t < NT; ++t) { const float e = __builtin_amdgcn_exp2f((sc[t] - m) * LOG2E); l += e; pp[t] = cvtpk(e, e); }
  l += dppf<ROR4>(l); l += dppf<ROR8>(l); l = xrow_sum(l);
  const int pid = br * 128 + h * NSPLIT + s;
  {
    unsigned old = 0;
    if (lane == 0) {
      *reinterpret_cast<f32x2*>(&lds[L_ML + 2 * w]) = f32x2{m, l};
      old = __hip_atomic_fetch_add(reinterpret_cast<unsigned*>(&lds[L_CNT]), 1u, __ATOMIC_ACQ_REL, __HIP_MEMORY_SCOPE_WORKGROUP);
    }
    if (__builtin_amdgcn_readfirstlane(old) == NW - 1) {
      const f32x2 e = *reinterpret_cast<const f32x2*>(&lds[L_ML + 2 * (lane & 7)]);
      float M = e[0];
      M = fmaxf(M, dppf<0xB1>(M)); M = fmaxf(M, dppf<0x4E>(M)); M = fmaxf(M, dppf<ROR4>(M));
      const float wt = __builtin_amdgcn_exp2f((e[0] - M) * LOG2E);
      float L = wt * e[1];
      L += dppf<0xB1>(L); L += dppf<0x4E>(L); L += dppf<ROR4>(L);
      if (lane < NW) lds[L_WT + lane] = wt;
      if (lane == 0) __builtin_amdgcn_raw_buffer_store_b128(v4u{__float_as_uint(M), tag, __float_as_uint(L), tag}, rw, WS_ML + pid * 16, 0, AUX_SC1);
    }
  }
  SB();

  f32x4 acc = {0.f, 0.f, 0.f, 0.f};
  sfor<NKL>([&](auto I) {
    constexpr int i = I, n = NKL + i;
    const unsigned pb = dppu<BCAST + 4 * (i & 3)>(pp[i >> 2]);
    const v4u a = {pb & mask[0], pb & mask[1], pb & mask[2], pb & mask[3]};
    acc = mfma(a, ring[n % W], acc);
    issue(std::integral_constant<int, n + W>{});
    SB();
  });
  if (g < 2) *reinterpret_cast<f32x4*>(&lds[L_ACC + w * 128 + 8 * c + 4 * g]) = acc;
  __syncthreads();

  float o = 0.f;
  {
    const f32x4 w0 = *reinterpret_cast<const f32x4*>(&lds[L_WT]), w1 = *reinterpret_cast<const f32x4*>(&lds[L_WT + 4]);
    const int row = w * (DH / NW) + 4 * ((c + so) & 3) + g;
    float o1 = 0.f;
#pragma unroll
    for (int ww = 0; ww < 4; ++ww) { o = fmaf(w0[ww], lds[L_ACC + ww * 128 + row], o); o1 = fmaf(w1[ww], lds[L_ACC + (ww + 4) * 128 + row], o1); }
    o += o1;
  }
  const unsigned ohi = cvtpk(o, o);
  const float ores = o - __uint_as_float(ohi & 0xffff0000u);
  const unsigned olo = cvtpk(ores, ores);
  unsigned mask2[4];
#pragma unroll
  for (int k = 0; k < 4; ++k) mask2[k] = (((c & 7) >> 1) == k) ? ((c & 1) ? 0xffff0000u : 0x0000ffffu) : 0u;
  f32x4 y[5];
#pragma unroll
  for (int cg = 0; cg < 5; ++cg) y[cg] = f32x4{0.f, 0.f, 0.f, 0.f};
  sfor<DH / NW / 4>([&](auto RG) {
    constexpr int rg = RG;
    const unsigned hb = dppu<BCAST + rg>(ohi), lb = dppu<BCAST + rg>(olo);
    const unsigned ob = c < 8 ? hb : lb;
    const v4u a = {ob & mask2[0], ob & mask2[1], ob & mask2[2], ob & mask2[3]};
    sfor<5>([&](auto CG) { y[CG] = mfma(a, aux[rg * 5 + CG], y[CG]); });
  });
#pragma unroll
  for (int cg = 0; cg < 5; ++cg) *reinterpret_cast<f32x4*>(&lds[L_Y + (2 * w + (g >> 1)) * DM + cg * 128 + 8 * c + 4 * (g & 1)]) = y[cg];
  __syncthreads();
  if (tid < DM / 2) {
    f32x2 t = {0.f, 0.f};
#pragma unroll
    for (int j = 0; j < 2 * NW; ++j) t += *reinterpret_cast<const f32x2*>(&lds[L_Y + j * DM + 2 * tid]);
    __builtin_amdgcn_raw_buffer_store_b128(v4u{__float_as_uint(t[0]), tag, __float_as_uint(t[1]), tag}, rw, WS_Y + (pid * 320 + tid) * 16, 0, AUX_SC1);
  }

  const int j = h * NSPLIT + s;
  if ((j & 7) < 5) {
    const int r = (j >> 3) * 5 + (j & 7);
    const int pr = br * 128 + (tid >> 2);
    const int yo = WS_Y + (pr * 320 + r * 4 + (tid & 3)) * 16, mo = WS_ML + pr * 16;
    v4u gy = {0u, 0u, 0u, 0u}, gm = {0u, 0u, 0u, 0u};
    bool oky = false, okm = false;
    for (unsigned spins = 0; spins < (1u << 20); ++spins) {
      if (!oky) gy = __builtin_amdgcn_raw_buffer_load_b128(rw, yo, 0, AUX_SC1);
      if (!okm) gm = __builtin_amdgcn_raw_buffer_load_b128(rw, mo, 0, AUX_SC1);
      oky = gy[1] == tag && gy[3] == tag; okm = gm[1] == tag && gm[3] == tag;
      if (__all(oky && okm)) break;
      __builtin_amdgcn_s_sleep(POLL_SLEEP);
      asm volatile("" ::: "memory");
    }
    const float mp = __uint_as_float(gm[0]);
    float Mh = fmaxf(mp, dppf<ROR4>(mp)); Mh = fmaxf(Mh, dppf<ROR8>(Mh));
    const float wt = __builtin_amdgcn_exp2f((mp - Mh) * LOG2E);
    float Lh = wt * __uint_as_float(gm[2]); Lh += dppf<ROR4>(Lh); Lh += dppf<ROR8>(Lh);
    const float f = wt / Lh;
    float t0 = __uint_as_float(gy[0]) * f, t1 = __uint_as_float(gy[2]) * f;
    t0 += dppf<ROR4>(t0); t1 += dppf<ROR4>(t1); t0 += dppf<ROR8>(t0); t1 += dppf<ROR8>(t1);
    t0 = xrow_sum(t0); t1 = xrow_sum(t1);
    if (lane < 4) *reinterpret_cast<f32x2*>(&lds[L_RED + w * 8 + 2 * lane]) = f32x2{t0, t1};
    __syncthreads();
    if (tid < 4) {
      f32x2 t = {0.f, 0.f};
#pragma unroll
      for (int ww = 0; ww < NW; ++ww) t += *reinterpret_cast<const f32x2*>(&lds[L_RED + ww * 8 + 2 * tid]);
      *reinterpret_cast<unsigned*>(outp + r * 8 + 2 * tid) = cvtpk(t[0], t[1]);
      if (r == 0 && tid == 0) __builtin_amdgcn_raw_buffer_store_b32(tag, rw, WS_EPOCH + br * 256, 0, AUX_SC1);
    }
  }
}

extern "C" void kernel_launch(void* const* d_in, const int* in_sizes, int n_in,
                              void* d_out, int out_size, void* d_ws, size_t ws_size,
                              hipStream_t stream) {
  Params p;
  for (int i = 0; i < 2; ++i) {
    p.x[i] = d_in[5 * i + 0]; p.k[i] = d_in[5 * i + 1]; p.v[i] = d_in[5 * i + 2]; p.wq[i] = d_in[5 * i + 3]; p.wo[i] = d_in[5 * i + 4];
  }
  p.ws = d_ws; p.out = (unsigned short*)d_out;
  hipLaunchKernelGGL(attn_fused, dim3(256), dim3(NTHR), 0, stream, p);
}
```

```cpp
#include <hip/hip_runtime.h>
#include <hip/hip_bf16.h>
#include <utility>

#define RING 20
#define KV_AUX 2
#define WQ_AUX 2
#define WO_AUX 0
#define PAT 3
#define POLL_SLEEP 4
#define POLL_S0 6
#define OROT 1
#define WO_PRE 0
#define QSLEEP 8
#define NPOLL 0

typedef unsigned v4u __attribute__((ext_vector_type(4)));
typedef float f32x4 __attribute__((ext_vector_type(4)));
typedef float f32x2 __attribute__((ext_vector_type(2)));
typedef short bf16x8 __attribute__((ext_vector_type(8)));
typedef __amdgpu_buffer_rsrc_t rsrc_t;

constexpr int DM = 640, NH = 32, SK = 8192, DH = 128;
constexpr int NSPLIT = 4, NW = 8, NTHR = NW * 64;
constexpr int NT = SK / NSPLIT / NW / 16;
constexpr int NQW = 4;
constexpr int NQL = DM / NSPLIT / NQW / 4;
constexpr int NKL = NT * 4;
constexpr int NOL = (DH / NW / 4) * 5;
constexpr int W = RING;
constexpr float SCALE = 0.088388347648318440f, LOG2E = 1.4426950408889634f;
constexpr int AUX_SC1 = 16;
constexpr int OOB = 1 << 30;
constexpr unsigned SPIN_CAP = 1u << 20;
constexpr int WS_EPOCH = 0, WS_ML = 4096, WS_Q = 8192, WS_Y = WS_Q + 256 * 64 * 16, WS_BYTES = WS_Y + 256 * 320 * 16;

struct Params {
  const void* x[2]; const void* k[2]; const void* v[2]; const void* wq[2]; const void* wo[2];
  void* ws; unsigned short* out;
};

template <int N, typename F, int... Is>
__device__ __forceinline__ void sfor_impl(F&& f, std::integer_sequence<int, Is...>) { (f(std::integral_constant<int, Is>{}), ...); }
template <int N, typename F> __device__ __forceinline__ void sfor(F&& f) { sfor_impl<N>(f, std::make_integer_sequence<int, N>{}); }

__device__ __forceinline__ rsrc_t mkrsrc(const void* p, int bytes) {
  return __builtin_amdgcn_make_buffer_rsrc(const_cast<void*>(p), 0, bytes, 0x00020000);
}
__device__ __forceinline__ f32x4 mfma(v4u a, v4u b, f32x4 c) {
  return __builtin_amdgcn_mfma_f32_16x16x32_bf16(__builtin_bit_cast(bf16x8, a), __builtin_bit_cast(bf16x8, b), c, 0, 0, 0);
}
template <int CTRL> __device__ __forceinline__ unsigned dppu(unsigned x) {
  return (unsigned)__builtin_amdgcn_mov_dpp((int)x, CTRL, 0xf, 0xf, true);
}
template <int CTRL> __device__ __forceinline__ float dppf(float x) {
  return __builtin_bit_cast(float, __builtin_amdgcn_mov_dpp(__builtin_bit_cast(int, x), CTRL, 0xf, 0xf, true));
}
__device__ __forceinline__ unsigned cvtpk(float lo, float hi) {
  typedef __bf16 bf2 __attribute__((ext_vector_type(2)));
  bf2 r; r[0] = (__bf16)lo; r[1] = (__bf16)hi;
  return __builtin_bit_cast(unsigned, r);
}
__device__ __forceinline__ float rbf(float x) { return __uint_as_float(cvtpk(0.f, x)); }
__device__ __forceinline__ float xrow_max(float x) {
  auto s = __builtin_amdgcn_permlane16_swap(__float_as_uint(x), __float_as_uint(x), false, false);
  x = fmaxf(__uint_as_float(s[0]), __uint_as_float(s[1]));
  auto t = __builtin_amdgcn_permlane32_swap(__float_as_uint(x), __float_as_uint(x), false, false);
  return fmaxf(__uint_as_float(t[0]), __uint_as_float(t[1]));
}
__device__ __forceinline__ float xrow_sum(float x) {
  auto s = __builtin_amdgcn_permlane16_swap(__float_as_uint(x), __float_as_uint(x), false, false);
  x = __uint_as_float(s[0]) + __uint_as_float(s[1]);
  auto t = __builtin_amdgcn_permlane32_swap(__float_as_uint(x), __float_as_uint(x), false, false);
  return __uint_as_float(t[0]) + __uint_as_float(t[1]);
}
__device__ __forceinline__ bool tagged(v4u g, unsigned tag) { return (g[1] == tag) & (g[3] == tag); }
#define SB() __builtin_amdgcn_sched_barrier(0)
constexpr int ROR4 = 0x124, ROR8 = 0x128, BCAST = 0x150;

constexpr int L_QP = 0;
constexpr int L_QB = L_QP + NW * 128;
constexpr int L_ACC = L_QB + 64;
constexpr int L_ML = L_ACC + NW * 128;
constexpr int L_WT = L_ML + 2 * NW;
constexpr int L_CNT = L_WT + NW;
constexpr int L_RED = L_CNT + 4;
constexpr int L_Y = L_RED + NW * 8;
constexpr int L_END = L_Y + NW * DM;

__global__ void __launch_bounds__(NTHR, 2) attn_fused(Params p) {
  __shared__ __attribute__((aligned(16))) float lds[L_END];
  const int b = blockIdx.x;
  const int hh = (b & 7) * 8 + (b >> 5), s = (b >> 3) & 3;
  const int br = hh >> 5, h = hh & 31;
  const int pid = hh * NSPLIT + s;
  const int tid = threadIdx.x, lane = tid & 63, w = __builtin_amdgcn_readfirstlane(tid >> 6);
  const int g = lane >> 4, c = lane & 15;

  const size_t kvoff = (size_t)h * SK * (DH * 2);
  const int rot = (int)((unsigned)(hh + 1) * 2654435761u >> 20);
  auto tile_off = [&](int k) {
    const int j = (k + rot) & 15;
    int t;
    if (PAT == 1) t = w | (s << 3) | (j << 5);
    else if (PAT == 2) t = (j & 1) | ((w & 1) << 1) | ((s & 1) << 2) | ((w & 2) << 2) | ((j >> 1) << 4) | ((s >> 1) << 7) | ((w >> 2) << 8);
    else t = w | ((j & 1) << 3) | ((s & 1) << 4) | (((j >> 1) & 3) << 5) | ((s >> 1) << 7) | ((j >> 3) << 8);
    return t * 4096;
  };
  const int qrow = (s * NQW + (w & (NQW - 1))) * (4 * NQL);
  const void *px = p.x[br], *pq = p.wq[br], *pk = p.k[br], *pv = p.v[br], *po = p.wo[br];
  unsigned short* outp = p.out;
  asm volatile("" : "+s"(px), "+s"(pq), "+s"(pk), "+s"(pv), "+s"(po), "+s"(outp));
  outp += br * DM;
  const rsrc_t rx = mkrsrc(px, DM * 2);
  const rsrc_t rq = mkrsrc((const char*)pq + (size_t)qrow * (NH * DH * 2) + h * (DH * 2), (4 * NQL - 1) * (NH * DH * 2) + DH * 2);
  const rsrc_t rk = mkrsrc((const char*)pk + kvoff, SK * DH * 2);
  const rsrc_t rv = mkrsrc((const char*)pv + kvoff, SK * DH * 2);
  const rsrc_t ro = mkrsrc((const char*)po + (size_t)(h * DH + w * (DH / NW)) * (DM * 2), (DH / NW) * DM * 2);
  const rsrc_t rw = mkrsrc(p.ws, WS_BYTES);

  const int q_lo = g * NQL * (NH * DH * 2) + c * 16;
  const int k_lo = (c >> 2) * 256 + g * 64 + (c & 3) * 16;
  const int v_lo = g * 1024 + c * 16;
  const int o_lo = g * (DM * 2) + c * 16;
  const int so = OROT ? s : 0;
  v4u ring[W], wo[NOL], wq[NQL];
  auto issue_o = [&](auto N) { constexpr int n = N;
    wo[n] = __builtin_amdgcn_raw_buffer_load_b128(ro, o_lo + (n % 5) * 256, (((n / 5) + so) & 3) * 4 * (DM * 2), WO_AUX); };
  auto issue = [&](auto N) {
    constexpr int n = N;
    if constexpr (n < NKL) ring[n % W] = __builtin_amdgcn_raw_buffer_load_b128(rk, k_lo + (n & 3) * 1024, tile_off(n >> 2), KV_AUX);
    else if constexpr (n < 2 * NKL) { constexpr int i = n - NKL; ring[n % W] = __builtin_amdgcn_raw_buffer_load_b128(rv, v_lo + (i & 3) * 256, tile_off(i >> 2), KV_AUX); }
  };
  unsigned mask[4];
  unsigned* const lu = reinterpret_cast<unsigned*>(lds);

  if (w >= NQW) {
    __syncthreads();
#pragma unroll
    for (int k = 0; k < 4; ++k) mask[k] = (c < 8 && (c >> 1) == k) ? ((c & 1) ? 0xffff0000u : 0x0000ffffu) : 0u;
  } else {
    sfor<NQL>([&](auto N) { wq[N] = __builtin_amdgcn_raw_buffer_load_b128(rq, q_lo, N * (NH * DH * 2), WQ_AUX); });
    unsigned x0 = __builtin_amdgcn_raw_buffer_load_b16(rx, (qrow + g * NQL + c) * 2, 0, 0);
    const unsigned epoch = __builtin_amdgcn_raw_buffer_load_b32(rw, WS_EPOCH + br * 256, 0, AUX_SC1);
    SB();
    *reinterpret_cast<v4u*>(&lds[L_CNT]) = v4u{0u, 0u, 0u, 0u};
    __syncthreads();
    SB();
#pragma unroll
    for (int k = 0; k < 4; ++k) mask[k] = (c < 8 && (c >> 1) == k) ? ((c & 1) ? 0xffff0000u : 0x0000ffffu) : 0u;
    x0 |= x0 << 16;
    f32x4 qa = {0.f, 0.f, 0.f, 0.f};
    sfor<NQL>([&](auto N) {
      const unsigned xb = dppu<BCAST + N>(x0);
      const v4u a = {xb & mask[0], xb & mask[1], xb & mask[2], xb & mask[3]};
      qa = mfma(a, wq[N], qa);
    });
    if (g < 2) *reinterpret_cast<f32x4*>(&lds[L_QP + w * 128 + 8 * c + 4 * g]) = qa;
    unsigned old = 0;
    if (lane == 0) old = __hip_atomic_fetch_add(reinterpret_cast<unsigned*>(&lds[L_CNT + 1]), 1u, __ATOMIC_ACQ_REL, __HIP_MEMORY_SCOPE_WORKGROUP);
    if (__builtin_amdgcn_readfirstlane(old) == NQW - 1) {
      __builtin_amdgcn_s_setprio(3);
      const unsigned tag = __builtin_amdgcn_readfirstlane(epoch) + 1u;
      f32x2 t = {0.f, 0.f};
#pragma unroll
      for (int ww = 0; ww < NQW; ++ww) t += *reinterpret_cast<const f32x2*>(&lds[L_QP + ww * 128 + 2 * lane]);
      __builtin_amdgcn_raw_buffer_store_b128(v4u{__float_as_uint(t[0]), tag, __float_as_uint(t[1]), tag}, rw, WS_Q + (pid * 64 + lane) * 16, 0, AUX_SC1);
      v4u g1, g2, g3;
#pragma nounroll
      for (unsigned spins = 0; spins < SPIN_CAP; ++spins) {
        __builtin_amdgcn_s_sleep(QSLEEP);
        g1 = __builtin_amdgcn_raw_buffer_load_b128(rw, WS_Q + ((pid ^ 1) * 64 + lane) * 16, 0, AUX_SC1);
        g2 = __builtin_amdgcn_raw_buffer_load_b128(rw, WS_Q + ((pid ^ 2) * 64 + lane) * 16, 0, AUX_SC1);
        g3 = __builtin_amdgcn_raw_buffer_load_b128(rw, WS_Q + ((pid ^ 3) * 64 + lane) * 16, 0, AUX_SC1);
        if (__all(tagged(g1, tag) & tagged(g2, tag) & tagged(g3, tag))) break;
        asm volatile("" ::: "memory");
      }
      const float q0 = (t[0] + __uint_as_float(g1[0])) + (__uint_as_float(g2[0]) + __uint_as_float(g3[0]));
      const float q1 = (t[1] + __uint_as_float(g1[2])) + (__uint_as_float(g2[2]) + __uint_as_float(g3[2]));
      lu[L_QB + lane] = cvtpk(q0, q1);
      lu[L_CNT + 3] = tag;
      __hip_atomic_store(&lu[L_CNT + 2], 1u, __ATOMIC_RELEASE, __HIP_MEMORY_SCOPE_WORKGROUP);
      __builtin_amdgcn_s_setprio(0);
    }
  }
  SB();
  sfor<W>([&](auto N) { issue(N); });
  SB();
  sfor<WO_PRE>([&](auto N) { issue_o(N); });
  SB();
#pragma nounroll
  while (__builtin_amdgcn_readfirstlane(__hip_atomic_load(&lu[L_CNT + 2], __ATOMIC_ACQUIRE, __HIP_MEMORY_SCOPE_WORKGROUP)) == 0u) __builtin_amdgcn_s_sleep(2);
  const unsigned tag = __builtin_amdgcn_readfirstlane(lu[L_CNT + 3]);
  v4u aq[4];
  {
    const v4u qf = *reinterpret_cast<const v4u*>(&lds[L_QB + 4 * (4 * g + (c & 3))]);
#pragma unroll
    for (int i = 0; i < 4; ++i) aq[i] = ((c >> 2) == i) ? qf : v4u{0u, 0u, 0u, 0u};
  }
  SB();

  float sc[NT];
  sfor<NT>([&](auto T) {
    constexpr int t = T;
    f32x4 d = {0.f, 0.f, 0.f, 0.f};
    sfor<4>([&](auto I) {
      constexpr int n = t * 4 + I;
      d = mfma(aq[I], ring[n % W], d);
      issue(std::integral_constant<int, n + W>{});
      if constexpr (n % 2 == 0 && n / 2 + WO_PRE < NOL) issue_o(std::integral_constant<int, n / 2 + WO_PRE>{});
      SB();
    });
    float v = (dppf<0x00>(d[0]) + dppf<0x55>(d[1])) + (dppf<0xAA>(d[2]) + dppf<0xFF>(d[3]));
    sc[t] = rbf(rbf(v) * SCALE);
  });
  float m = sc[0];
#pragma unroll
  for (int t = 1; t < NT; ++t) m = fmaxf(m, sc[t]);
  m = fmaxf(m, dppf<ROR4>(m)); m = fmaxf(m, dppf<ROR8>(m)); m = xrow_max(m);
  float l = 0.f;
  unsigned pp[NT];
#pragma unroll
  for (int t = 0; t < NT; ++t) { const float e = __builtin_amdgcn_exp2f((sc[t] - m) * LOG2E); l += e; pp[t] = cvtpk(e, e); }
  l += dppf<ROR4>(l); l += dppf<ROR8>(l); l = xrow_sum(l);
  {
    unsigned old = 0;
    if (lane == 0) {
      *reinterpret_cast<f32x2*>(&lds[L_ML + 2 * w]) = f32x2{m, l};
      old = __hip_atomic_fetch_add(reinterpret_cast<unsigned*>(&lds[L_CNT]), 1u, __ATOMIC_ACQ_REL, __HIP_MEMORY_SCOPE_WORKGROUP);
    }
    if (__builtin_amdgcn_readfirstlane(old) == NW - 1) {
      const f32x2 e = *reinterpret_cast<const f32x2*>(&lds[L_ML + 2 * (lane & 7)]);
      float M = e[0];
      M = fmaxf(M, dppf<0xB1>(M)); M = fmaxf(M, dppf<0x4E>(M)); M = fmaxf(M, dppf<ROR4>(M));
      const float wt = __builtin_amdgcn_exp2f((e[0] - M) * LOG2E);
      float L = wt * e[1];
      L += dppf<0xB1>(L); L += dppf<0x4E>(L); L += dppf<ROR4>(L);
      if (lane < NW) lds[L_WT + lane] = wt;
      if (lane == 0) __builtin_amdgcn_raw_buffer_store_b128(v4u{__float_as_uint(M), tag, __float_as_uint(L), tag}, rw, WS_ML + pid * 16, 0, AUX_SC1);
    }
  }
  SB();

  f32x4 acc = {0.f, 0.f, 0.f, 0.f};
  sfor<NKL>([&](auto I) {
    constexpr int i = I, n = NKL + i;
    const unsigned pb = dppu<BCAST + 4 * (i & 3)>(pp[i >> 2]);
    const v4u a = {pb & mask[0], pb & mask[1], pb & mask[2], pb & mask[3]};
    acc = mfma(a, ring[n % W], acc);
    issue(std::integral_constant<int, n + W>{});
    SB();
  });
  if (g < 2) *reinterpret_cast<f32x4*>(&lds[L_ACC + w * 128 + 8 * c + 4 * g]) = acc;
  __syncthreads();

  const int j = pid & 127;
  const bool reducer = (j & 7) < 5;
  const int r = (j >> 3) * 5 + (j & 7);
  const int pr = br * 128 + (tid >> 2);
  const int yo = WS_Y + (pr * 320 + r * 4 + (tid & 3)) * 16, mo = WS_ML + pr * 16;

  float o = 0.f;
  {
    const f32x4 w0 = *reinterpret_cast<const f32x4*>(&lds[L_WT]), w1 = *reinterpret_cast<const f32x4*>(&lds[L_WT + 4]);
    const int row = w * (DH / NW) + 4 * ((c + so) & 3) + g;
    float o1 = 0.f;
#pragma unroll
    for (int ww = 0; ww < 4; ++ww) { o = fmaf(w0[ww], lds[L_ACC + ww * 128 + row], o); o1 = fmaf(w1[ww], lds[L_ACC + (ww + 4) * 128 + row], o1); }
    o += o1;
  }
  const unsigned ob = cvtpk(o, o);
  f32x4 y[5];
#pragma unroll
  for (int cg = 0; cg < 5; ++cg) y[cg] = f32x4{0.f, 0.f, 0.f, 0.f};
  sfor<DH / NW / 4>([&](auto RG) {
    constexpr int rg = RG;
    const unsigned hb = dppu<BCAST + rg>(ob);
    const v4u a = {hb & mask[0], hb & mask[1], hb & mask[2], hb & mask[3]};
    sfor<5>([&](auto CG) { y[CG] = mfma(a, wo[rg * 5 + CG], y[CG]); });
  });
  if (g < 2) {
#pragma unroll
    for (int cg = 0; cg < 5; ++cg) *reinterpret_cast<f32x4*>(&lds[L_Y + w * DM + cg * 128 + 8 * c + 4 * g]) = y[cg];
  }
  __syncthreads();
  {
    f32x2 t = {0.f, 0.f}, u = {0.f, 0.f};
    if (w < DM / 128) {
#pragma unroll
      for (int ww = 0; ww < NW; ww += 2) { t += *reinterpret_cast<const f32x2*>(&lds[L_Y + ww * DM + 2 * tid]); u += *reinterpret_cast<const f32x2*>(&lds[L_Y + (ww + 1) * DM + 2 * tid]); }
      t += u;
    }
    __builtin_amdgcn_raw_buffer_store_b128(v4u{__float_as_uint(t[0]), tag, __float_as_uint(t[1]), tag}, rw, w < DM / 128 ? WS_Y + (pid * 320 + tid) * 16 : OOB, 0, AUX_SC1);
  }

  if (reducer) {
    float t0 = 0.f, t1 = 0.f;
    v4u gy = {0u, 0u, 0u, 0u}, gm = {0u, 0u, 0u, 0u};
    {
      bool oky = false, okm = false;
#pragma nounroll
      for (unsigned spins = 0; spins < SPIN_CAP; ++spins) {
        if (!oky) gy = __builtin_amdgcn_raw_buffer_load_b128(rw, yo, 0, AUX_SC1);
        if (!okm) gm = __builtin_amdgcn_raw_buffer_load_b128(rw, mo, 0, AUX_SC1);
        oky = tagged(gy, tag); okm = tagged(gm, tag);
        if (__all(oky && okm)) break;
        __builtin_amdgcn_s_sleep(POLL_SLEEP);
        asm volatile("" ::: "memory");
      }
    }
    const float mp = __uint_as_float(gm[0]);
    float Mh = fmaxf(mp, dppf<ROR4>(mp)); Mh = fmaxf(Mh, dppf<ROR8>(Mh));
    const float wt = __builtin_amdgcn_exp2f((mp - Mh) * LOG2E);
    float Lh = wt * __uint_as_float(gm[2]); Lh += dppf<ROR4>(Lh); Lh += dppf<ROR8>(Lh);
    float f = wt / Lh;
    t0 = __uint_as_float(gy[0]); t1 = __uint_as_float(gy[2]);
    t0 *= f; t1 *= f;
    t0 += dppf<ROR4>(t0); t1 += dppf<ROR4>(t1); t0 += dppf<ROR8>(t0); t1 += dppf<ROR8>(t1);
    t0 = xrow_sum(t0); t1 = xrow_sum(t1);
    if (lane < 4) *reinterpret_cast<f32x2*>(&lds[L_RED + w * 8 + 2 * lane]) = f32x2{t0, t1};
    __syncthreads();
    if (tid < 4) {
      f32x2 t = {0.f, 0.f};
#pragma unroll
      for (int ww = 0; ww < NW; ++ww) t += *reinterpret_cast<const f32x2*>(&lds[L_RED + ww * 8 + 2 * tid]);
      ((__attribute__((address_space(1))) unsigned*)(outp + r * 8))[tid] = cvtpk(t[0], t[1]);
      if (r == 0 && tid == 0) __builtin_amdgcn_raw_buffer_store_b32(tag, rw, WS_EPOCH + br * 256, 0, AUX_SC1);
    }
  }
}

extern "C" void kernel_launch(void* const* d_in, const int* in_sizes, int n_in,
                              void* d_out, int out_size, void* d_ws, size_t ws_size,
                              hipStream_t stream) {
  Params p;
  for (int i = 0; i < 2; ++i) {
    p.x[i] = d_in[5 * i + 0]; p.k[i] = d_in[5 * i + 1]; p.v[i] = d_in[5 * i + 2]; p.wq[i] = d_in[5 * i + 3]; p.wo[i] = d_in[5 * i + 4];
  }
  p.ws = d_ws; p.out = (unsigned short*)d_out;
  hipLaunchKernelGGL(attn_fused, dim3(256), dim3(NTHR), 0, stream, p);
}
```

```cpp
#include <hip/hip_runtime.h>
#include <hip/hip_bf16.h>
#include <utility>

#define RING 20
#define DEPTH 3
#define T0 100
#define T1 245
#define D1 108
#define TWO 690
#define T5 790
#define T31 3620
#define STG 108
#define QEARLY 0
#define POLL1 3
#define BAR0 1
#define WO_RATE 2
#define GATE 245
#define KV_AUX 2
#define WQ_AUX 2
#define WO_AUX 0
#define POLL_SLEEP 4
#define OROT 1
#define QSLEEP 4
#define CPF 0
#define CSH 6

typedef unsigned v4u __attribute__((ext_vector_type(4)));
typedef unsigned v2u __attribute__((ext_vector_type(2)));
typedef float f32x4 __attribute__((ext_vector_type(4)));
typedef float f32x2 __attribute__((ext_vector_type(2)));
typedef short bf16x8 __attribute__((ext_vector_type(8)));
typedef __amdgpu_buffer_rsrc_t rsrc_t;

constexpr int DM = 640, NH = 32, SK = 8192, DH = 128;
constexpr int NSPLIT = 4, NW = 8, NTHR = NW * 64;
constexpr int NT = SK / NSPLIT / NW / 16;
constexpr int NQL = DM / NSPLIT / NW / 4;
constexpr int NKL = NT * 4;
constexpr int NOL = (DH / NW / 4) * 5;
constexpr int W = RING;
constexpr float SCALE = 0.088388347648318440f, LOG2E = 1.4426950408889634f;
constexpr int AUX_SC1 = 16;
constexpr int OOB = 1 << 30;
constexpr unsigned SPIN_CAP = 1u << 20;
constexpr int WS_EPOCH = 0, WS_ML = 4096, WS_Q = 8192, WS_Y = WS_Q + 256 * 64 * 16, WS_BYTES = WS_Y + 256 * 320 * 16;

struct Params {
  const void* x[2]; const void* k[2]; const void* v[2]; const void* wq[2]; const void* wo[2];
  void* ws; unsigned short* out;
};

template <int N, typename F, int... Is>
__device__ __forceinline__ void sfor_impl(F&& f, std::integer_sequence<int, Is...>) { (f(std::integral_constant<int, Is>{}), ...); }
template <int N, typename F> __device__ __forceinline__ void sfor(F&& f) { sfor_impl<N>(f, std::make_integer_sequence<int, N>{}); }

__device__ __forceinline__ rsrc_t mkrsrc(const void* p, int bytes) {
  return __builtin_amdgcn_make_buffer_rsrc(const_cast<void*>(p), 0, bytes, 0x00020000);
}
__device__ __forceinline__ f32x4 mfma(v4u a, v4u b, f32x4 c) {
  return __builtin_amdgcn_mfma_f32_16x16x32_bf16(__builtin_bit_cast(bf16x8, a), __builtin_bit_cast(bf16x8, b), c, 0, 0, 0);
}
template <int CTRL> __device__ __forceinline__ unsigned dppu(unsigned x) {
  return (unsigned)__builtin_amdgcn_mov_dpp((int)x, CTRL, 0xf, 0xf, true);
}
template <int CTRL> __device__ __forceinline__ float dppf(float x) {
  return __builtin_bit_cast(float, __builtin_amdgcn_mov_dpp(__builtin_bit_cast(int, x), CTRL, 0xf, 0xf, true));
}
__device__ __forceinline__ unsigned cvtpk(float lo, float hi) {
  typedef __bf16 bf2 __attribute__((ext_vector_type(2)));
  bf2 r; r[0] = (__bf16)lo; r[1] = (__bf16)hi;
  return __builtin_bit_cast(unsigned, r);
}
__device__ __forceinline__ float rbf(float x) { return __uint_as_float(cvtpk(0.f, x)); }
__device__ __forceinline__ float xrow_max(float x) {
  auto s = __builtin_amdgcn_permlane16_swap(__float_as_uint(x), __float_as_uint(x), false, false);
  x = fmaxf(__uint_as_float(s[0]), __uint_as_float(s[1]));
  auto t = __builtin_amdgcn_permlane32_swap(__float_as_uint(x), __float_as_uint(x), false, false);
  return fmaxf(__uint_as_float(t[0]), __uint_as_float(t[1]));
}
__device__ __forceinline__ float xrow_sum(float x) {
  auto s = __builtin_amdgcn_permlane16_swap(__float_as_uint(x), __float_as_uint(x), false, false);
  x = __uint_as_float(s[0]) + __uint_as_float(s[1]);
  auto t = __builtin_amdgcn_permlane32_swap(__float_as_uint(x), __float_as_uint(x), false, false);
  return __uint_as_float(t[0]) + __uint_as_float(t[1]);
}
__device__ __forceinline__ void wait_until(unsigned due) {
  asm volatile(
      "1:\n\t"
      "s_memrealtime s[70:71]\n\t"
      "s_waitcnt lgkmcnt(0)\n\t"
      "s_sub_u32 s70, s70, %0\n\t"
      "s_cmp_ge_i32 s70, 0\n\t"
      "s_cbranch_scc1 2f\n\t"
      "s_sleep 1\n\t"
      "s_branch 1b\n"
      "2:"
      :: "s"(due) : "s70", "s71", "scc");
}
__device__ __forceinline__ bool tagged(v4u g, unsigned tag) { return (g[1] == tag) & (g[3] == tag); }
#define SB() __builtin_amdgcn_sched_barrier(0)
constexpr int ROR4 = 0x124, ROR8 = 0x128, BCAST = 0x150;

constexpr int L_QP = 0;
constexpr int L_QS = L_QP + NW * 128;
constexpr int L_ACC = L_QS + NSPLIT * 128;
constexpr int L_ML = L_ACC + NW * 128;
constexpr int L_WT = L_ML + 2 * NW;
constexpr int L_CNT = L_WT + NW;
constexpr int L_RED = L_CNT + 4;
constexpr int L_Y = L_RED + NW * 8;
constexpr int L_END = L_Y + NW * DM;

__global__ void __launch_bounds__(NTHR, 2) attn_fused(Params p) {
  __shared__ __attribute__((aligned(16))) float lds[L_END];
  unsigned cpf = 0;
  if (CPF) {
    asm volatile("s_getpc_b64 s[70:71]\n\tv_lshlrev_b32 %0, %2, %1\n\tv_min_u32 %0, %3, %0\n\tglobal_load_dword %0, %0, s[70:71]"
                 : "=&v"(cpf) : "v"(threadIdx.x), "n"(CSH), "n"(CPF) : "s70", "s71");
    SB();
  }
  const int b = blockIdx.x;
  const int hh = (b & 7) * 8 + (b >> 5), s = (b >> 3) & 3;
  const int br = hh >> 5, h = hh & 31;
  const int pid = hh * NSPLIT + s;
  const int tid = threadIdx.x, lane = tid & 63, w = __builtin_amdgcn_readfirstlane(tid >> 6);
  const int g = lane >> 4, c = lane & 15;

  const size_t kvoff = (size_t)h * SK * (DH * 2);
  auto tile_off = [&](int k) {
    const int j = (k ^ hh) & 15;
    return (w | ((j & 1) << 3) | ((s & 1) << 4) | (((j >> 1) & 3) << 5) | ((s >> 1) << 7) | ((j >> 3) << 8)) * 4096;
  };
  const int qrow = (s * NW + w) * (4 * NQL);
  const void *px = p.x[br], *pq = p.wq[br], *pk = p.k[br], *pv = p.v[br], *po = p.wo[br];
  unsigned short* outp = p.out;
  unsigned long long t64 = __builtin_amdgcn_s_memrealtime();
  asm volatile("" : "+s"(px), "+s"(pq), "+s"(pk), "+s"(pv), "+s"(po), "+s"(outp), "+s"(t64));
  outp += br * DM;
  const rsrc_t rx = mkrsrc(px, DM * 2);
  const rsrc_t rq = mkrsrc((const char*)pq + (size_t)qrow * (NH * DH * 2) + h * (DH * 2), (4 * NQL - 1) * (NH * DH * 2) + DH * 2);
  const rsrc_t rk = mkrsrc((const char*)pk + kvoff, SK * DH * 2);
  const rsrc_t rv = mkrsrc((const char*)pv + kvoff, SK * DH * 2);
  const rsrc_t ro = mkrsrc((const char*)po + (size_t)(h * DH + w * (DH / NW)) * (DM * 2), (DH / NW) * DM * 2);
  const rsrc_t rw = mkrsrc(p.ws, WS_BYTES);

  const int q_lo = g * NQL * (NH * DH * 2) + c * 16;
  const int k_lo = (c >> 2) * 256 + g * 64 + (c & 3) * 16;
  const int v_lo = g * 1024 + c * 16;
  const int o_lo = g * (DM * 2) + c * 16;
  const int so = OROT ? s : 0;
  v4u ring[W], wo[NOL], wq[NQL];
  auto issue_o = [&](auto N) { constexpr int n = N;
    wo[n] = __builtin_amdgcn_raw_buffer_load_b128(ro, o_lo + (n % 5) * 256, (((n / 5) + so) & 3) * 4 * (DM * 2), WO_AUX); };
  auto issue = [&](auto N) {
    constexpr int n = N;
    if constexpr (n < NKL) ring[n % W] = __builtin_amdgcn_raw_buffer_load_b128(rk, k_lo + (n & 3) * 1024, tile_off(n >> 2), KV_AUX);
    else if constexpr (n < 2 * NKL) { constexpr int i = n - NKL; ring[n % W] = __builtin_amdgcn_raw_buffer_load_b128(rv, v_lo + (i & 3) * 256, tile_off(i >> 2), KV_AUX); }
  };
  constexpr int NP = W / 4;
  const unsigned t_start = (unsigned)t64;
  struct Tab { constexpr __device__ int operator[](int k) const {
    return k == 0 ? T0 : k < NP ? T1 + (k - 1) * D1 : k < 2 * NT ? T5 + (T31 - T5) * (k - NP) / (2 * NT - 1 - NP) : k == 32 ? TWO : k == 33 ? STG : T31 + GATE; } };
  constexpr Tab tab;
  unsigned slot;
  auto pace = [&](auto K) {
    constexpr int k = K;
    wait_until(slot + tab[k]);
    if constexpr (k >= NP && k < 32) __builtin_amdgcn_s_barrier();
    SB();
  };
  auto round = [&](auto K) { sfor<4>([&](auto I) { issue(std::integral_constant<int, K * 4 + I>{}); }); SB(); };
  auto next = [&](auto N) {
    constexpr int m = N + 4 * DEPTH;
    if constexpr (m >= W && m < 2 * NKL) {
      if constexpr (m % 4 == 0) pace(std::integral_constant<int, m / 4>{});
      issue(std::integral_constant<int, m>{});
      constexpr int e = 4 / WO_RATE;
      if constexpr ((m - W) % e == 0 && 5 + (m - W) / e < NOL) issue_o(std::integral_constant<int, 5 + (m - W) / e>{});
    }
    SB();
  };
  unsigned* const lu = reinterpret_cast<unsigned*>(lds);

  sfor<NQL>([&](auto N) { wq[N] = __builtin_amdgcn_raw_buffer_load_b128(rq, q_lo, N * (NH * DH * 2), WQ_AUX); });
  unsigned x0 = __builtin_amdgcn_raw_buffer_load_b16(rx, (qrow + g * NQL + c) * 2, 0, 0);
  const unsigned epoch = __builtin_amdgcn_raw_buffer_load_b32(rw, w < NSPLIT ? WS_EPOCH + br * 256 : OOB, 0, AUX_SC1);
  SB();
  if (BAR0) __builtin_amdgcn_s_barrier();
  SB();
  slot = t_start + (((b >> 3) * tab[33]) >> 5);
  pace(std::integral_constant<int, 0>{});
  round(std::integral_constant<int, 0>{});
  unsigned mask[4];
#pragma unroll
  for (int k = 0; k < 4; ++k) mask[k] = (c < 8 && (c >> 1) == k) ? ((c & 1) ? 0xffff0000u : 0x0000ffffu) : 0u;
  lu[L_CNT] = 0u;
  if (!QEARLY) {
    pace(std::integral_constant<int, 1>{});
    round(std::integral_constant<int, 1>{});
  }
  {
    x0 |= x0 << 16;
    f32x4 qa = {0.f, 0.f, 0.f, 0.f};
    sfor<NQL>([&](auto N) {
      const unsigned xb = dppu<BCAST + N>(x0);
      const v4u a = {xb & mask[0], xb & mask[1], xb & mask[2], xb & mask[3]};
      qa = mfma(a, wq[N], qa);
    });
    *reinterpret_cast<f32x4*>(&lds[(g < 2 ? L_QP : L_Y - 8) + w * 128 + 8 * c + 4 * g]) = qa;
  }
  unsigned tag = __builtin_amdgcn_readfirstlane(epoch) + 1u;
  if (CPF) asm volatile("" :: "v"(cpf), "s"(tag));
  __syncthreads();
  v4u gq;
  {
    f32x2 t = {0.f, 0.f};
#pragma unroll
    for (int ww = 0; ww < NW; ++ww) t += *reinterpret_cast<const f32x2*>(&lds[L_QP + ww * 128 + 2 * lane]);
    gq = v4u{__float_as_uint(t[0]), tag, __float_as_uint(t[1]), tag};
    __builtin_amdgcn_raw_buffer_store_b128(gq, rw, w == s ? WS_Q + (pid * 64 + lane) * 16 : OOB, 0, AUX_SC1);
  }
  SB();
  bool have = w == s || w >= NSPLIT;
  const int qo = WS_Q + ((pid - s + w) * 64 + lane) * 16;
  v4u pend;
  sfor<NP - 1 + QEARLY>([&](auto I) {
    constexpr int k = I + 2 - QEARLY;
    pace(std::integral_constant<int, k < NP ? k : 32>{});
    if constexpr (k > POLL1) { if (!have) gq = pend; have = have || __all(tagged(pend, tag)); }
    if constexpr (k >= POLL1 && k < NP) pend = __builtin_amdgcn_raw_buffer_load_b128(rw, have ? OOB : qo, 0, AUX_SC1);
    SB();
    if constexpr (k < NP) round(std::integral_constant<int, k>{}); else sfor<5>([&](auto N) { issue_o(N); });
    SB();
  });
  if (!have) {
    const v4u rwv = {(unsigned)(size_t)p.ws, (unsigned)((size_t)p.ws >> 32) & 0xffffu, (unsigned)WS_BYTES, 0x00020000u};
#pragma nounroll
    for (unsigned spins = 0; spins < SPIN_CAP; ++spins) {
      asm volatile("buffer_load_dwordx4 %0, %1, %2, 0 offen sc1\n\ts_waitcnt vmcnt(0)" : "=&v"(gq) : "v"(qo), "s"(rwv) : "memory");
      if (__all(tagged(gq, tag))) break;
      __builtin_amdgcn_s_sleep(QSLEEP);
    }
  }
  if (w < NSPLIT) {
    *reinterpret_cast<v2u*>(&lds[L_QS + w * 128 + 2 * lane]) = v2u{gq[0], gq[2]};
    lu[L_CNT + 3] = tag;
  }
  __syncthreads();
  tag = __builtin_amdgcn_readfirstlane(lu[L_CNT + 3]);
  v4u aq[4];
  {
    const int e = L_QS + 8 * (4 * g + (c & 3));
    f32x4 u[2];
#pragma unroll
    for (int i = 0; i < 2; ++i)
      u[i] = (*reinterpret_cast<const f32x4*>(&lds[e + 4 * i]) + *reinterpret_cast<const f32x4*>(&lds[e + 128 + 4 * i])) +
             (*reinterpret_cast<const f32x4*>(&lds[e + 256 + 4 * i]) + *reinterpret_cast<const f32x4*>(&lds[e + 384 + 4 * i]));
    const v4u qf = {cvtpk(u[0][0], u[0][1]), cvtpk(u[0][2], u[0][3]), cvtpk(u[1][0], u[1][1]), cvtpk(u[1][2], u[1][3])};
#pragma unroll
    for (int i = 0; i < 4; ++i) aq[i] = ((c >> 2) == i) ? qf : v4u{0u, 0u, 0u, 0u};
  }
  SB();

  float sc[NT];
  sfor<NT>([&](auto T) {
    constexpr int t = T;
    f32x4 d = {0.f, 0.f, 0.f, 0.f};
    sfor<4>([&](auto I) {
      constexpr int n = t * 4 + I;
      d = mfma(aq[I], ring[n % W], d);
      SB();
      next(std::integral_constant<int, n>{});
    });
    float v = (dppf<0x00>(d[0]) + dppf<0x55>(d[1])) + (dppf<0xAA>(d[2]) + dppf<0xFF>(d[3]));
    sc[t] = rbf(rbf(v) * SCALE);
  });
  float m = sc[0];
#pragma unroll
  for (int t = 1; t < NT; ++t) m = fmaxf(m, sc[t]);
  m = fmaxf(m, dppf<ROR4>(m)); m = fmaxf(m, dppf<ROR8>(m)); m = xrow_max(m);
  float l = 0.f;
  unsigned pp[NT];
#pragma unroll
  for (int t = 0; t < NT; ++t) { const float e = __builtin_amdgcn_exp2f((sc[t] - m) * LOG2E); l += e; pp[t] = cvtpk(e, e); }
  l += dppf<ROR4>(l); l += dppf<ROR8>(l); l = xrow_sum(l);
  {
    unsigned old = 0;
    if (lane == 0) {
      *reinterpret_cast<f32x2*>(&lds[L_ML + 2 * w]) = f32x2{m, l};
      old = __hip_atomic_fetch_add(reinterpret_cast<unsigned*>(&lds[L_CNT]), 1u, __ATOMIC_ACQ_REL, __HIP_MEMORY_SCOPE_WORKGROUP);
    }
    if (__builtin_amdgcn_readfirstlane(old) == NW - 1) {
      const f32x2 e = *reinterpret_cast<const f32x2*>(&lds[L_ML + 2 * (lane & 7)]);
      float M = e[0];
      M = fmaxf(M, dppf<0xB1>(M)); M = fmaxf(M, dppf<0x4E>(M)); M = fmaxf(M, dppf<ROR4>(M));
      const float wt = __builtin_amdgcn_exp2f((e[0] - M) * LOG2E);
      float L = wt * e[1];
      L += dppf<0xB1>(L); L += dppf<0x4E>(L); L += dppf<ROR4>(L);
      if (lane < NW) lds[L_WT + lane] = wt;
      if (lane == 0) __builtin_amdgcn_raw_buffer_store_b128(v4u{__float_as_uint(M), tag, __float_as_uint(L), tag}, rw, WS_ML + pid * 16, 0, AUX_SC1);
    }
  }
  SB();

  const int j = pid & 127;
  const bool reducer = (j & 31) < 20;
  const int r = (j >> 5) * 20 + (j & 31);
  const int pr = br * 128 + g * 32 + w * 4 + (c >> 2);
  const int yo = WS_Y + (pr * 320 + r * 4 + (tid & 3)) * 16, mo = WS_ML + pr * 16;
  v4u gm;

  f32x4 acc = {0.f, 0.f, 0.f, 0.f};
  sfor<NKL>([&](auto I) {
    constexpr int i = I, n = NKL + i;
    const unsigned pb = dppu<BCAST + 4 * (i & 3)>(pp[i >> 2]);
    const v4u a = {pb & mask[0], pb & mask[1], pb & mask[2], pb & mask[3]};
    acc = mfma(a, ring[n % W], acc);
    SB();
    if constexpr (n + 4 * DEPTH == 2 * NKL - 5) gm = __builtin_amdgcn_raw_buffer_load_b128(rw, reducer ? mo : OOB, 0, AUX_SC1);
    next(std::integral_constant<int, n>{});
  });
  if (g < 2) *reinterpret_cast<f32x4*>(&lds[L_ACC + w * 128 + 8 * c + 4 * g]) = acc;
  __syncthreads();

  float o = 0.f;
  {
    const f32x4 w0 = *reinterpret_cast<const f32x4*>(&lds[L_WT]), w1 = *reinterpret_cast<const f32x4*>(&lds[L_WT + 4]);
    const int row = w * (DH / NW) + 4 * ((c + so) & 3) + g;
    float o1 = 0.f;
#pragma unroll
    for (int ww = 0; ww < 4; ++ww) { o = fmaf(w0[ww], lds[L_ACC + ww * 128 + row], o); o1 = fmaf(w1[ww], lds[L_ACC + (ww + 4) * 128 + row], o1); }
    o += o1;
  }
  const unsigned ob = cvtpk(o, o);
  f32x4 y[5];
#pragma unroll
  for (int cg = 0; cg < 5; ++cg) y[cg] = f32x4{0.f, 0.f, 0.f, 0.f};
  sfor<DH / NW / 4>([&](auto RG) {
    constexpr int rg = RG;
    const unsigned hb = dppu<BCAST + rg>(ob);
    const v4u a = {hb & mask[0], hb & mask[1], hb & mask[2], hb & mask[3]};
    sfor<5>([&](auto CG) { y[CG] = mfma(a, wo[rg * 5 + CG], y[CG]); });
  });
  if (g < 2) {
#pragma unroll
    for (int cg = 0; cg < 5; ++cg) *reinterpret_cast<f32x4*>(&lds[L_Y + w * DM + cg * 128 + 8 * c + 4 * g]) = y[cg];
  }
  __syncthreads();
  {
    f32x2 t = {0.f, 0.f}, u = {0.f, 0.f};
    if (w < DM / 128) {
#pragma unroll
      for (int ww = 0; ww < NW; ww += 2) { t += *reinterpret_cast<const f32x2*>(&lds[L_Y + ww * DM + 2 * tid]); u += *reinterpret_cast<const f32x2*>(&lds[L_Y + (ww + 1) * DM + 2 * tid]); }
      t += u;
    }
    __builtin_amdgcn_raw_buffer_store_b128(v4u{__float_as_uint(t[0]), tag, __float_as_uint(t[1]), tag}, rw, w < DM / 128 ? WS_Y + (pid * 320 + tid) * 16 : OOB, 0, AUX_SC1);
  }

  if (reducer) {
    float t0 = 0.f, t1 = 0.f;
#pragma nounroll
    for (unsigned spins = 0; spins < SPIN_CAP && !__all(tagged(gm, tag)); ++spins) {
      __builtin_amdgcn_s_sleep(POLL_SLEEP);
      gm = __builtin_amdgcn_raw_buffer_load_b128(rw, mo, 0, AUX_SC1);
      asm volatile("" ::: "memory");
    }
    const float mp = __uint_as_float(gm[0]);
    float Mh = fmaxf(mp, dppf<ROR4>(mp)); Mh = fmaxf(Mh, dppf<ROR8>(Mh));
    const float wt = __builtin_amdgcn_exp2f((mp - Mh) * LOG2E);
    float Lh = wt * __uint_as_float(gm[2]); Lh += dppf<ROR4>(Lh); Lh += dppf<ROR8>(Lh);
    float f = wt / Lh;
    asm volatile("" : "+v"(f));
    wait_until(t_start + (((4 * w + 3) * tab[33]) >> 5) + tab[34]);
    v4u gy = {0u, 0u, 0u, 0u};
    {
      bool oky = false;
#pragma nounroll
      for (unsigned spins = 0; spins < SPIN_CAP; ++spins) {
        if (!oky) gy = __builtin_amdgcn_raw_buffer_load_b128(rw, yo, 0, AUX_SC1);
        oky = tagged(gy, tag);
        if (__all(oky)) break;
        __builtin_amdgcn_s_sleep(POLL_SLEEP);
        asm volatile("" ::: "memory");
      }
    }
    t0 = __uint_as_float(gy[0]); t1 = __uint_as_float(gy[2]);
    t0 *= f; t1 *= f;
    t0 += dppf<ROR4>(t0); t1 += dppf<ROR4>(t1); t0 += dppf<ROR8>(t0); t1 += dppf<ROR8>(t1);
    t0 = xrow_sum(t0); t1 = xrow_sum(t1);
    if (lane < 4) *reinterpret_cast<f32x2*>(&lds[L_RED + w * 8 + 2 * lane]) = f32x2{t0, t1};
    __syncthreads();
    if (tid < 4) {
      f32x2 t = {0.f, 0.f};
#pragma unroll
      for (int ww = 0; ww < NW; ++ww) t += *reinterpret_cast<const f32x2*>(&lds[L_RED + ww * 8 + 2 * tid]);
      ((__attribute__((address_space(1))) unsigned*)(outp + r * 8))[tid] = cvtpk(t[0], t[1]);
      if (r == 0 && tid == 0) __builtin_amdgcn_raw_buffer_store_b32(tag, rw, WS_EPOCH + br * 256, 0, AUX_SC1);
    }
  }
}

extern "C" void kernel_launch(void* const* d_in, const int* in_sizes, int n_in,
                              void* d_out, int out_size, void* d_ws, size_t ws_size,
                              hipStream_t stream) {
  Params p;
  for (int i = 0; i < 2; ++i) {
    p.x[i] = d_in[5 * i + 0]; p.k[i] = d_in[5 * i + 1]; p.v[i] = d_in[5 * i + 2]; p.wq[i] = d_in[5 * i + 3]; p.wo[i] = d_in[5 * i + 4];
  }
  p.ws = d_ws; p.out = (unsigned short*)d_out;
  hipLaunchKernelGGL(attn_fused, dim3(256), dim3(NTHR), 0, stream, p);
}
```

```cpp
#include <hip/hip_runtime.h>
#include <hip/hip_bf16.h>
#include <utility>

typedef unsigned v8u __attribute__((ext_vector_type(8)));
typedef unsigned v4u __attribute__((ext_vector_type(4)));
typedef unsigned v2u __attribute__((ext_vector_type(2)));
typedef float f32x4 __attribute__((ext_vector_type(4)));
typedef float f32x2 __attribute__((ext_vector_type(2)));
typedef short s16x4 __attribute__((ext_vector_type(4)));
typedef __bf16 bf16x8 __attribute__((ext_vector_type(8)));
typedef __bf16 bf16x16 __attribute__((ext_vector_type(16)));
typedef __amdgpu_buffer_rsrc_t rsrc_t;

constexpr int DM = 640, NH = 32, SK = 8192, DH = 128;
constexpr int NSPLIT = 4, NW = 8, NTHR = NW * 64;
constexpr int NT = SK / NSPLIT / NW / 16;
constexpr int NQL = DM / NSPLIT / NW / 4;
constexpr int NKL = NT * 4;
constexpr int NOL = (DH / NW / 4) * 5;
constexpr int NG = DM / 4;
constexpr int W = 20, DEPTH = 3;
constexpr int T0 = 100, T1 = 245, D1 = 108, TWO = 690, T5 = 790, T31 = 3630, STG = 108, GATE = 212;
constexpr int POLL1 = 3;
constexpr float SCALE = 0.088388347648318440f, LOG2E = 1.4426950408889634f;
constexpr int AUX_NT = 2, AUX_SC1 = 16;
constexpr int OOB = 1 << 30;
constexpr unsigned SPIN_CAP = 1u << 20, TAG_STEP = 0x9E3779B9u;
constexpr int WS_EPOCH = 0, WS_ML = 4096, WS_Q = 8192, WS_Y = WS_Q + 256 * 32 * 16, WS_BYTES = WS_Y + 256 * NG * 16;

struct Params {
  const void* x[2]; const void* k[2]; const void* v[2]; const void* wq[2]; const void* wo[2];
  void* ws; unsigned short* out;
};

template <int N, typename F, int... Is>
__device__ __forceinline__ void sfor_impl(F&& f, std::integer_sequence<int, Is...>) { (f(std::integral_constant<int, Is>{}), ...); }
template <int N, typename F> __device__ __forceinline__ void sfor(F&& f) { sfor_impl<N>(f, std::make_integer_sequence<int, N>{}); }

__device__ __forceinline__ rsrc_t mkrsrc(const void* p, int bytes) {
  return __builtin_amdgcn_make_buffer_rsrc(const_cast<void*>(p), 0, bytes, 0x00020000);
}
__device__ __forceinline__ f32x4 mfma(v4u a, v4u b, f32x4 c) {
  return __builtin_amdgcn_mfma_f32_16x16x32_bf16(__builtin_bit_cast(bf16x8, a), __builtin_bit_cast(bf16x8, b), c, 0, 0, 0);
}
__device__ __forceinline__ f32x4 smfma(v4u a, v8u b, f32x4 c, int idx) {
  return __builtin_amdgcn_smfmac_f32_16x16x64_bf16(__builtin_bit_cast(bf16x8, a), __builtin_bit_cast(bf16x16, b), c, idx, 0, 0);
}
template <int CTRL> __device__ __forceinline__ unsigned dppu(unsigned x) {
  return (unsigned)__builtin_amdgcn_mov_dpp((int)x, CTRL, 0xf, 0xf, true);
}
template <int CTRL> __device__ __forceinline__ float dppf(float x) {
  return __builtin_bit_cast(float, __builtin_amdgcn_mov_dpp(__builtin_bit_cast(int, x), CTRL, 0xf, 0xf, true));
}
__device__ __forceinline__ unsigned cvtpk(float lo, float hi) {
  typedef __bf16 bf2 __attribute__((ext_vector_type(2)));
  bf2 r; r[0] = (__bf16)lo; r[1] = (__bf16)hi;
  return __builtin_bit_cast(unsigned, r);
}
__device__ __forceinline__ float rbf(float x) { return __uint_as_float(cvtpk(0.f, x)); }
__device__ __forceinline__ float xrow_max(float x) {
  auto s = __builtin_amdgcn_permlane16_swap(__float_as_uint(x), __float_as_uint(x), false, false);
  x = fmaxf(__uint_as_float(s[0]), __uint_as_float(s[1]));
  auto t = __builtin_amdgcn_permlane32_swap(__float_as_uint(x), __float_as_uint(x), false, false);
  return fmaxf(__uint_as_float(t[0]), __uint_as_float(t[1]));
}
__device__ __forceinline__ float fold16(float ev, float od) {
  auto t = __builtin_amdgcn_permlane16_swap(__float_as_uint(ev), __float_as_uint(od), false, false);
  return __uint_as_float(t[0]) + __uint_as_float(t[1]);
}
__device__ __forceinline__ float fold32(float lo, float hi) {
  auto t = __builtin_amdgcn_permlane32_swap(__float_as_uint(lo), __float_as_uint(hi), false, false);
  return __uint_as_float(t[0]) + __uint_as_float(t[1]);
}
__device__ __forceinline__ void wait_until(unsigned due) {
  asm volatile(
      "1:\n\t"
      "s_memrealtime s[70:71]\n\t"
      "s_waitcnt lgkmcnt(0)\n\t"
      "s_sub_u32 s70, s70, %0\n\t"
      "s_cmp_ge_i32 s70, 0\n\t"
      "s_cbranch_scc1 2f\n\t"
      "s_sleep 1\n\t"
      "s_branch 1b\n"
      "2:"
      :: "s"(due) : "s70", "s71", "scc");
}
__device__ __forceinline__ unsigned check(v4u g, unsigned tag) {
  return __builtin_amdgcn_bitop3_b32(g[0] ^ tag, __builtin_amdgcn_alignbit(g[1], g[1], 11), __builtin_amdgcn_alignbit(g[2], g[2], 21), 0x96);
}
__device__ __forceinline__ bool tagged(v4u g, unsigned tag) { return g[3] == check(g, tag); }
__device__ __forceinline__ v4u pack4(f32x4 t, unsigned tag) {
  const v4u u = __builtin_bit_cast(v4u, t) + 0x80u;
  v4u g = {__builtin_amdgcn_perm(u[0], u[3], 0x07060501u), __builtin_amdgcn_perm(u[1], u[3], 0x07060502u), __builtin_amdgcn_perm(u[2], u[3], 0x07060503u), 0u};
  g[3] = check(g, tag);
  return g;
}
__device__ __forceinline__ f32x4 unpack4(v4u g) {
  const unsigned d = __builtin_amdgcn_perm(__builtin_amdgcn_perm(g[2], g[1], 0x04000c0cu), g[0], 0x0706000cu);
  return __builtin_bit_cast(f32x4, v4u{g[0] & 0xffffff00u, g[1] & 0xffffff00u, g[2] & 0xffffff00u, d});
}
#define SB() __builtin_amdgcn_sched_barrier(0)
#define PIN(x) asm volatile("" : "+v"(x))
constexpr int XOR1 = 0xB1, XOR2 = 0x4E, SHL = 0x100, ROR4 = 0x124, ROR8 = 0x128, HMIR = 0x141, BCAST = 0x150;

constexpr int L_QP = 0;
constexpr int L_QS = L_QP + NW * 128;
constexpr int L_M = L_QS + NSPLIT * 128;
constexpr int L_L = L_M + NW;
constexpr int L_TAG = L_L + NW;
constexpr int L_RED = L_TAG + 4;
constexpr int L_ST = L_RED + NW * 64;
constexpr int ST_ROW = DM / 2 + 8;
constexpr int L_ACC = L_ST, ACC_ROW = 520;
constexpr int L_END = L_ST + 32 * ST_ROW;

__global__ void __launch_bounds__(NTHR, 2) attn_fused(Params p) {
  __shared__ __attribute__((aligned(16))) float lds[L_END];
  const int b = blockIdx.x;
  const int br = (b >> 2) & 1, h = (b & 3) * 8 + (b >> 5), s = (b >> 3) & 3;
  const int pid = (b >> 5) * 32 + (b & 7) * 4 + s;
  const int tid = threadIdx.x, lane = tid & 63, w = __builtin_amdgcn_readfirstlane(tid >> 6);
  const int g = lane >> 4, c = lane & 15;

  const size_t kvoff = (size_t)h * SK * (DH * 2);
  auto tile_off = [&](int k) {
    const int j = (k ^ h) & 15;
    return (w | ((j & 1) << 3) | ((s & 1) << 4) | (((j >> 1) & 3) << 5) | ((s >> 1) << 7) | ((j >> 3) << 8)) * 4096;
  };
  const int qrow = (s * NW + w) * (4 * NQL);
  const void *px = p.x[br], *pq = p.wq[br], *pk = p.k[br], *pv = p.v[br], *po = p.wo[br];
  unsigned short* outp = p.out;
  unsigned long long t64 = __builtin_amdgcn_s_memrealtime();
  asm volatile("" : "+s"(px), "+s"(pq), "+s"(pk), "+s"(pv), "+s"(po), "+s"(outp), "+s"(t64));
  const rsrc_t rx = mkrsrc(px, DM * 2);
  const rsrc_t rq = mkrsrc((const char*)pq + (size_t)qrow * (NH * DH * 2) + h * (DH * 2), (4 * NQL - 1) * (NH * DH * 2) + DH * 2);
  const rsrc_t rk = mkrsrc((const char*)pk + kvoff, SK * DH * 2);
  const rsrc_t rv = mkrsrc((const char*)pv + kvoff, SK * DH * 2);
  const rsrc_t ro = mkrsrc((const char*)po + (size_t)(h * DH + w * (DH / NW)) * (DM * 2), (DH / NW) * DM * 2);
  const rsrc_t rw = mkrsrc(p.ws, WS_BYTES);
  const rsrc_t rout = mkrsrc(outp, 2 * DM * 2);

  const int q_lo = g * NQL * (NH * DH * 2) + c * 16;
  const int k_lo = (c >> 2) * 256 + g * 64 + (c & 3) * 16;
  const int v_lo = g * 1024 + c * 16;
  const int o_lo = g * (DM * 2) + c * 16;
  v4u ring[W], wo[NOL], wq[NQL];
  v8u wb[2][5];
  auto issue_o = [&](auto N) { constexpr int n = N;
    wo[n] = __builtin_amdgcn_raw_buffer_load_b128(ro, o_lo + (n % 5) * 256, (((n / 5) + s) & 3) * 4 * (DM * 2), 0); };
  auto issue = [&](auto N) {
    constexpr int n = N;
    if constexpr (n < NKL) ring[n % W] = __builtin_amdgcn_raw_buffer_load_b128(rk, k_lo + (n & 3) * 1024, tile_off(n >> 2), AUX_NT);
    else ring[n % W] = __builtin_amdgcn_raw_buffer_load_b128(rv, v_lo + (n & 3) * 256, tile_off(n >> 2), AUX_NT);
  };
  constexpr int NP = W / 4;
  const unsigned t_start = (unsigned)t64;
  struct Tab { constexpr __device__ int operator[](int k) const {
    return k == 0 ? T0 : k < NP ? T1 + (k - 1) * D1 : k < 2 * NT ? T5 + (T31 - T5) * (k - NP) / (2 * NT - 1 - NP) : k == 32 ? TWO : k == 33 ? STG : T31 + GATE; } };
  constexpr Tab tab;
  unsigned slot, tag;
  float M;
  unsigned* const lu = reinterpret_cast<unsigned*>(lds);
  auto lds4 = [&](int at) -> f32x4& { return *reinterpret_cast<f32x4*>(&lds[at]); };
  auto pace = [&](auto K) {
    constexpr int k = K;
    wait_until(slot + tab[k]);
    if constexpr (k >= NP && k < 32) __syncthreads();
    SB();
  };
  auto round = [&](auto K) { sfor<4>([&](auto I) { issue(std::integral_constant<int, K * 4 + I>{}); }); SB(); };
  auto between = [&](auto K) {
    constexpr int k = K;
    sfor<4>([&](auto S) {
      constexpr int st = S, kw = NP + (5 * st - 1) / 2 + DEPTH;
      if constexpr (k == kw) sfor<5>([&](auto I) { lds4(L_ST + (4 * w + g) * ST_ROW + I * 64 + c * 4) = __builtin_bit_cast(f32x4, wo[st * 5 + I]); });
      if constexpr (k == kw + 1) sfor<10>([&](auto I) {
        constexpr int t = I / 2, q = I % 2;
        typedef __attribute__((address_space(3))) s16x4 l16x4;
        const v2u r = __builtin_bit_cast(v2u, __builtin_amdgcn_ds_read_tr16_b64_v4i16(
            (l16x4*)&lds[L_ST + (16 * q + (lane >> 2)) * ST_ROW + 40 * w + 8 * t + 2 * (lane & 3)]));
        wb[st / 2][t][4 * (st % 2) + 2 * q] = r[0]; wb[st / 2][t][4 * (st % 2) + 2 * q + 1] = r[1];
      });
    });
    if constexpr (k == NT + DEPTH) {
      float L = lds[L_L + (lane & 7)];
      L += dppf<XOR1>(L); L += dppf<XOR2>(L); L += dppf<HMIR>(L);
      v4u gr = {__float_as_uint(M), __float_as_uint(L), tag, 0u};
      gr[3] = check(gr, tag);
      __builtin_amdgcn_raw_buffer_store_b128(gr, rw, tid == 0 ? WS_ML + pid * 16 : OOB, 0, AUX_SC1);
    }
    SB();
  };
  auto next = [&](auto N) {
    constexpr int m = N + 4 * DEPTH;
    if constexpr (m >= W && m < 2 * NKL) {
      if constexpr (m % 4 == 0) pace(std::integral_constant<int, m / 4>{});
      issue(std::integral_constant<int, m>{});
      if constexpr ((m - W) % 2 == 0 && 5 + (m - W) / 2 < NOL) issue_o(std::integral_constant<int, 5 + (m - W) / 2>{});
      SB();
      if constexpr (m % 4 == 3) between(std::integral_constant<int, m / 4>{});
    }
    SB();
  };

  sfor<NQL>([&](auto N) { wq[N] = __builtin_amdgcn_raw_buffer_load_b128(rq, q_lo, N * (NH * DH * 2), AUX_NT); });
  unsigned x0 = __builtin_amdgcn_raw_buffer_load_b16(rx, (qrow + g * NQL + c) * 2, 0, 0);
  const unsigned epoch = __builtin_amdgcn_raw_buffer_load_b32(rw, w < 2 ? WS_EPOCH + br * 256 : OOB, 0, AUX_SC1);
  SB();
  __builtin_amdgcn_s_barrier();
  SB();
  slot = t_start + (((b >> 3) * tab[33]) >> 5);
  pace(std::integral_constant<int, 0>{});
  round(std::integral_constant<int, 0>{});
  unsigned mask[4];
#pragma unroll
  for (int k = 0; k < 4; ++k) mask[k] = (c < 8 && (c >> 1) == k) ? ((c & 1) ? 0xffff0000u : 0x0000ffffu) : 0u;
  auto diag = [&](unsigned x) { return v4u{x & mask[0], x & mask[1], x & mask[2], x & mask[3]}; };
  lu[L_TAG + 1] = 0u;
  pace(std::integral_constant<int, 1>{});
  round(std::integral_constant<int, 1>{});
  {
    x0 |= x0 << 16;
    f32x4 qa = {0.f, 0.f, 0.f, 0.f};
    sfor<NQL>([&](auto N) { qa = mfma(diag(dppu<BCAST + N>(x0)), wq[N], qa); });
    lds4((g < 2 ? L_QP : L_ST - 8) + w * 128 + 8 * c + 4 * g) = qa;
  }
  tag = __builtin_amdgcn_readfirstlane(epoch) + TAG_STEP;
  __syncthreads();
  {
    const int at = L_QP + (lane >> 5) * 512 + (lane & 31) * 4;
    const f32x4 t = (lds4(at) + lds4(at + 128)) + (lds4(at + 256) + lds4(at + 384));
    __builtin_amdgcn_raw_buffer_store_b128(pack4(f32x4{fold32(t[0], t[0]), fold32(t[1], t[1]), fold32(t[2], t[2]), fold32(t[3], t[3])}, tag),
                                           rw, tid < 32 ? WS_Q + (pid * 32 + lane) * 16 : OOB, 0, AUX_SC1);
  }
  SB();
  v4u gq, pend;
  bool have = w >= 2;
  const int qo = WS_Q + ((pid - s) * 32 + tid) * 16;
  sfor<NP - 1>([&](auto I) {
    constexpr int k = I + 2;
    pace(std::integral_constant<int, k < NP ? k : 32>{});
    if constexpr (k > POLL1) { if (!have) gq = pend; have = have || __all(tagged(pend, tag)); }
    if constexpr (k >= POLL1 && k < NP) pend = __builtin_amdgcn_raw_buffer_load_b128(rw, have ? OOB : qo, 0, AUX_SC1);
    SB();
    if constexpr (k < NP) round(std::integral_constant<int, k>{}); else sfor<5>([&](auto N) { issue_o(N); });
    SB();
  });
  const v4u rwv = {(unsigned)(size_t)p.ws, (unsigned)((size_t)p.ws >> 32) & 0xffffu, (unsigned)WS_BYTES, 0x00020000u};
  if (!have) {
#pragma nounroll
    for (unsigned spins = 0; spins < SPIN_CAP; ++spins) {
      asm volatile("buffer_load_dwordx4 %0, %1, %2, 0 offen sc1\n\ts_waitcnt vmcnt(0)" : "=&v"(gq) : "v"(qo), "s"(rwv) : "memory");
      if (__all(tagged(gq, tag))) break;
      __builtin_amdgcn_s_sleep(4);
    }
  }
  if (w < 2) {
    lds4(L_QS + 4 * tid) = unpack4(gq);
    lu[L_TAG] = tag;
  }
  __syncthreads();
  tag = __builtin_amdgcn_readfirstlane(lu[L_TAG]);
  v4u aq[4];
  {
    const int e = L_QS + 8 * (4 * g + (c & 3));
    f32x4 u[2];
#pragma unroll
    for (int i = 0; i < 2; ++i) u[i] = (lds4(e + 4 * i) + lds4(e + 128 + 4 * i)) + (lds4(e + 256 + 4 * i) + lds4(e + 384 + 4 * i));
    const v4u qf = {cvtpk(u[0][0], u[0][1]), cvtpk(u[0][2], u[0][3]), cvtpk(u[1][0], u[1][1]), cvtpk(u[1][2], u[1][3])};
#pragma unroll
    for (int i = 0; i < 4; ++i) aq[i] = ((c >> 2) == i) ? qf : v4u{0u, 0u, 0u, 0u};
  }
  SB();

  float sc[NT];
  sfor<NT>([&](auto T) {
    constexpr int t = T;
    f32x4 d = {0.f, 0.f, 0.f, 0.f};
    sfor<4>([&](auto I) {
      constexpr int n = t * 4 + I;
      d = mfma(aq[I], ring[n % W], d);
      SB();
      next(std::integral_constant<int, n>{});
    });
    const float v = (dppf<0x00>(d[0]) + dppf<0x55>(d[1])) + (dppf<0xAA>(d[2]) + dppf<0xFF>(d[3]));
    sc[t] = rbf(rbf(v) * SCALE);
  });
  unsigned pp[NT];
  {
    float m = sc[0];
#pragma unroll
    for (int t = 1; t < NT; ++t) m = fmaxf(m, sc[t]);
    m = fmaxf(m, dppf<ROR4>(m)); m = fmaxf(m, dppf<ROR8>(m));
    lds[L_M + w] = xrow_max(m);
    __syncthreads();
    M = lds[L_M + (lane & 7)];
    M = fmaxf(M, dppf<XOR1>(M)); M = fmaxf(M, dppf<XOR2>(M)); M = fmaxf(M, dppf<HMIR>(M));
    float l = 0.f;
#pragma unroll
    for (int t = 0; t < NT; ++t) { const float e = __builtin_amdgcn_exp2f((sc[t] - M) * LOG2E); l += e; pp[t] = cvtpk(e, e); }
    l += dppf<ROR4>(l); l += dppf<ROR8>(l); l = fold16(l, l);
    lds[L_L + w] = fold32(l, l);
  }
  SB();

  const bool reducer = b < NG, live = reducer && lane < 32;
  const int rb = (b >> 3) * 4 + (b & 3);
  const int pr = w * 32 + ((lane >> 3) + 4 * br) * 4 + (lane & 3);
  int mo = live ? WS_ML + pr * 16 : OOB, yo = live ? WS_Y + (pr * NG + 2 * rb + ((lane >> 2) & 1)) * 16 : OOB;
  constexpr unsigned long long IDLE = 0xffffffff00000000ull;
  v4u gm;
  float f;
  const int wsrc = 2 * (g & 1) + ((c >> 2) & 1) + 4 * ((c >> 1) & 1);
  typedef __attribute__((address_space(3))) float lf32;
  typedef __attribute__((address_space(3))) f32x2 lf32x2;
  typedef __attribute__((address_space(3))) f32x4 lf32x4;
  lf32x4* a_put = (lf32x4*)&lds[L_ACC + w * ACC_ROW + (g < 2 ? 8 * c + 4 * (g ^ (c >> 3)) : 4 * lane)];
  lf32x2* a_get = (lf32x2*)&lds[L_ACC + ((16 * wsrc + 4 * ((2 * (c >> 3) + (g >> 1) + s) & 3) + 2 * (c & 1)) ^ (wsrc & 4))];
  int y_put = g == 0 ? WS_Y + (pid * NG + 20 * w + c) * 16 : g == 1 && (c & 6) == 0 ? WS_Y + (pid * NG + 20 * w + 16 + (c >> 2) + (c & 1)) * 16 : OOB;
  int idx = (c & 1) ? 0xEEEE : 0x4444;
  const int u10 = rb % 10, vi = 2 * (c >> 3) + (g & 1);
  int o_put = (br * DM + 80 * (rb / 10) + (u10 < 8 ? 2 : 8) * u10 + (u10 < 8 ? 16 : 2) * vi) * 2 + (lane & 39) * 4096;
  int e_put = rb == 0 && tid == 0 ? WS_EPOCH + br * 256 : OOB;
  lf32* r_at = (lf32*)&lds[L_RED + lane];
  PIN(o_put); PIN(e_put); PIN(r_at);
  PIN(mo); PIN(yo); PIN(a_put); PIN(a_get); PIN(y_put); PIN(idx);
  f32x4 y[5];

  f32x4 acc = {0.f, 0.f, 0.f, 0.f};
  sfor<NKL>([&](auto I) {
    constexpr int i = I, n = NKL + i;
    acc = mfma(diag(dppu<BCAST + 4 * (i & 3)>(pp[i >> 2])), ring[n % W], acc);
    PIN(acc);
    SB();
    if constexpr (i == NKL - 29) gm = __builtin_amdgcn_raw_buffer_load_b128(rw, mo, 0, AUX_SC1);
    if constexpr (i == NKL - 12) {
      if (reducer && (__builtin_amdgcn_ballot_w64(tagged(gm, tag) && gm[2] == tag) | IDLE) != ~0ull) {
#pragma nounroll
        for (unsigned spins = 0; spins < SPIN_CAP; ++spins) {
          asm volatile("buffer_load_dwordx4 %0, %1, %2, 0 offen sc1\n\ts_waitcnt vmcnt(0)" : "=&v"(gm) : "v"(mo), "s"(rwv) : "memory");
          if ((__builtin_amdgcn_ballot_w64(tagged(gm, tag) && gm[2] == tag) | IDLE) == ~0ull) break;
          __builtin_amdgcn_s_sleep(4);
        }
      }
      const float mp = __uint_as_float(gm[0]);
      float Mh = fmaxf(mp, dppf<XOR1>(mp)); Mh = fmaxf(Mh, dppf<XOR2>(Mh));
      const float wt = __builtin_amdgcn_exp2f((mp - Mh) * LOG2E);
      float Lh = wt * __uint_as_float(gm[1]); Lh += dppf<XOR1>(Lh); Lh += dppf<XOR2>(Lh);
      f = wt / Lh;
      PIN(f);
    }
    if constexpr (i == NKL - 9) sfor<5>([&](auto T) { y[T] = f32x4{0.f, 0.f, 0.f, 0.f}; PIN(y[T]); });
    next(std::integral_constant<int, n>{});
  });
  *a_put = acc;
  __syncthreads();

  {
    f32x2 o[4];
#pragma unroll
    for (int i = 0; i < 4; ++i) o[i] = a_get[i * ACC_ROW] + a_get[i * ACC_ROW + ACC_ROW / 2];
    const f32x2 os = (o[0] + o[1]) + (o[2] + o[3]);
    const unsigned ob = cvtpk(os[0], os[1]);
    const v4u a0 = {ob, dppu<SHL + 2>(ob), dppu<SHL + 4>(ob), dppu<SHL + 6>(ob)};
    y[4] = smfma(a0, wb[0][4], y[4], idx);
    SB();
    const v4u a1 = {dppu<SHL + 8>(ob), dppu<SHL + 10>(ob), dppu<SHL + 12>(ob), dppu<SHL + 14>(ob)};
    y[3] = smfma(a0, wb[0][3], y[3], idx);
    SB();
    y[4] = smfma(a1, wb[1][4], y[4], idx);
    y[3] = smfma(a1, wb[1][3], y[3], idx);
    SB();
    sfor<3>([&](auto T) { y[T] = smfma(a0, wb[0][T], y[T], idx); });
    SB();
    const unsigned y4 = __float_as_uint(y[4][0] + y[4][1]);
    const int y4r = __builtin_amdgcn_permlane16_swap(y4, y4, false, false)[0];
    unsigned u[4];
    auto pick = [&](auto I) { constexpr int i = I;
      u[i] = __builtin_amdgcn_update_dpp(__builtin_bit_cast(int, y[i][0] + y[i][1]), y4r, i ? SHL + 2 * i : 0xE4, 0x2, 0xf, false) + 0x80u; };
    y[1] = smfma(a1, wb[1][1], y[1], idx);
    SB();
    pick(std::integral_constant<int, 3>{});
    y[2] = smfma(a1, wb[1][2], y[2], idx);
    y[0] = smfma(a1, wb[1][0], y[0], idx);
    SB();
    v4u gr;
    pick(std::integral_constant<int, 1>{}); gr[1] = __builtin_amdgcn_perm(u[1], u[3], 0x07060502u);
    pick(std::integral_constant<int, 2>{}); gr[2] = __builtin_amdgcn_perm(u[2], u[3], 0x07060503u);
    unsigned ck = check(v4u{0u, gr[1], gr[2], 0u}, tag);
    PIN(ck);
    SB();
    pick(std::integral_constant<int, 0>{}); gr[0] = __builtin_amdgcn_perm(u[0], u[3], 0x07060501u);
    gr[3] = ck ^ gr[0];
    __builtin_amdgcn_raw_buffer_store_b128(gr, rw, y_put, 0, AUX_SC1);
    __builtin_amdgcn_raw_buffer_store_b32(tag, rw, e_put, 0, AUX_SC1);
  }

  if (reducer) {
    wait_until(t_start + (((4 * w + 3) * tab[33]) >> 5) + tab[34]);
    v4u gy;
    float r[NW - 1];
    unsigned arrived;
    auto look = [&]() {
      arrived = __hip_atomic_load(&lu[L_TAG + 1], __ATOMIC_RELAXED, __HIP_MEMORY_SCOPE_WORKGROUP);
      asm volatile("" ::: "memory");
#pragma unroll
      for (int i = 0; i < NW - 1; ++i) r[i] = r_at[i * 64];
      asm volatile("" ::: "memory");
    };
    bool ok = false;
#pragma nounroll
    for (unsigned spins = 0; spins < SPIN_CAP; ++spins) {
      if (!ok) gy = __builtin_amdgcn_raw_buffer_load_b128(rw, yo, 0, AUX_SC1);
      asm volatile("" : "+v"(gy) :: "memory");
      look();
      unsigned long long m = __builtin_amdgcn_ballot_w64(tagged(gy, tag)) | IDLE;
      if (m == ~0ull) break;
      m &= (m >> 4) & 0x0f0f0f0f0f0f0f0full; m |= m << 4;
      ok = __builtin_amdgcn_inverse_ballot_w64(m);
      __builtin_amdgcn_s_sleep(1);
    }
    const f32x4 u = f * unpack4(gy);
    const float lo = fold16(u[0], u[1]), hi = fold16(u[2], u[3]);
    float t = ((c & 8) ? hi : lo) + dppf<ROR8>((c & 8) ? lo : hi);
    t += dppf<XOR1>(t); t += dppf<XOR2>(t);
    if (w < NW - 1) {
      r_at[w * 64] = t;
      if (lane == 0) asm volatile("ds_add_u32 %0, %1" :: "v"((lf32*)&lds[L_TAG + 1]), "v"(1) : "memory");
    } else {
      while (__builtin_amdgcn_readfirstlane(arrived) != NW - 1) look();
      t += ((r[0] + r[1]) + (r[2] + r[3])) + ((r[4] + r[5]) + r[6]);
      __builtin_amdgcn_raw_buffer_store_b32(cvtpk(t, dppf<SHL + 4>(t)), rout, o_put, 0, 0);
    }
  }
}

extern "C" void kernel_launch(void* const* d_in, const int* in_sizes, int n_in,
                              void* d_out, int out_size, void* d_ws, size_t ws_size,
                              hipStream_t stream) {
  Params p;
  for (int i = 0; i < 2; ++i) {
    p.x[i] = d_in[5 * i + 0]; p.k[i] = d_in[5 * i + 1]; p.v[i] = d_in[5 * i + 2]; p.wq[i] = d_in[5 * i + 3]; p.wo[i] = d_in[5 * i + 4];
  }
  p.ws = d_ws; p.out = (unsigned short*)d_out;
  hipLaunchKernelGGL(attn_fused, dim3(256), dim3(NTHR), 0, stream, p);
}
```
